```python
import torch

def _softmax(x, axis=-1):
    return torch.softmax(x, dim=axis)
def _gelu(x):
    return torch.nn.functional.gelu(x, approximate="tanh")
def _silu(x):
    return torch.nn.functional.silu(x)
def _sigmoid(x):
    return torch.sigmoid(x)
def _layernorm(x, axis=-1, eps=1e-5):
    m = x.mean(dim=axis, keepdim=True)
    v = x.var(dim=axis, keepdim=True, unbiased=False)
    return (x - m) / torch.sqrt(v + eps)
def _scatter_add(tgt, idx, src, axis=0):
    return tgt.clone().index_add_(axis, idx, src)
def _segment_sum(src, seg, n):
    out = torch.zeros((n,) + src.shape[1:], dtype=src.dtype)
    return out.index_add_(0, seg, src)
def _pad(x, pw):
    flat = tuple(v for pair in reversed(pw) for v in pair)
    return torch.nn.functional.pad(x, flat)

kernel_name = 'concat_heads__ssm_conv_s4d__spectral_gfnet__prompt'

def setup_inputs():

    _w = (lambda a: (lambda t: t.to(torch.bfloat16) if t.is_floating_point() else t)(torch.as_tensor(a)))
    def _A_setup():
        Bsz, L, D, N = 1, 2048, 1024, 128
        return {
            "u": _w((lambda *s: torch.randn(*s, dtype=torch.bfloat16) * 0.05)(Bsz, L, D)),
            "log_A_real": _w((lambda *s: torch.randn(*s, dtype=torch.bfloat16) * 6.4)(D, N) * 0.5),
            "A_imag": _w((lambda *s: torch.randn(*s, dtype=torch.bfloat16) * 0.05)(D, N) * 3.14),
            "C_real": _w((lambda *s: torch.randn(*s, dtype=torch.bfloat16) * 0.05)(D, N) / N ** 0.5),
            "C_imag": _w((lambda *s: torch.randn(*s, dtype=torch.bfloat16) * 0.05)(D, N) / N ** 0.5),
            "log_dt": _w((lambda *s: torch.randn(*s, dtype=torch.bfloat16) * 0.05)(D) * 0.5 - 4.0),
            "D_skip": _w((lambda *s: torch.randn(*s, dtype=torch.bfloat16) * 0.05)(D)),
        }
    _A_in = _A_setup()

    def _B_setup():
        B, S, D = 1, 2048, 1024
        Df = D // 2 + 1
        return {
            "x": _w((lambda *s: torch.randn(*s, dtype=torch.bfloat16) * 0.05)(B, S, D)),
            "w_r": _w((lambda *s: torch.randn(*s, dtype=torch.bfloat16) * 0.8)(S, Df) * 0.02),
            "w_i": _w((lambda *s: torch.randn(*s, dtype=torch.bfloat16) * 0.8)(S, Df) * 0.02),
        }
    _B_in = _B_setup()

    return {
        "A_u": _A_in["u"],
        "A_log_A_real": _A_in["log_A_real"],
        "A_A_imag": _A_in["A_imag"],
        "A_C_real": _A_in["C_real"],
        "A_C_imag": _A_in["C_imag"],
        "A_log_dt": _A_in["log_dt"],
        "A_D_skip": _A_in["D_skip"],
        "B_x": _B_in["x"],
        "B_w_r": _B_in["w_r"],
        "B_w_i": _B_in["w_i"],
    }

def reference(A_u, A_log_A_real, A_A_imag, A_C_real, A_C_imag, A_log_dt, A_D_skip, B_x, B_w_r, B_w_i):
    def _A_ref(u, log_A_real, A_imag, C_real, C_imag, log_dt, D_skip):
        L = u.shape[1]
        dt = torch.exp(log_dt)
        A = -torch.exp(log_A_real) + 1j * A_imag
        C = (C_real + 1j * C_imag) * dt[:, None]
        pos = (lambda x, dt: x.to(dt))(torch.arange(L), log_dt.dtype)
        P = torch.exp((A * dt[:, None])[:, :, None] * pos[None, None, :])
        K = 2.0 * torch.einsum("dn,dnl->dl", C, P).real
        L2 = 2 * L
        u_t = (lambda x, *d: x.permute(*d))(u, 0, 2, 1)
        Kf = (lambda x, *a, **k: torch.fft.rfft(x.float(), *a, **k))(K, L2)
        Uf = (lambda x, *a, **k: torch.fft.rfft(x.float(), *a, **k))(u_t, L2)
        y = (lambda x, *a, **k: torch.fft.irfft(x, *a, **k).to(torch.bfloat16))(Uf * Kf[None, :, :], L2)[:, :, :L]
        y = (lambda x, *d: x.permute(*d))(y, 0, 2, 1)
        return y + u * D_skip[None, None, :]

    def _B_ref(x, w_r, w_i):
        B, S, D = x.shape
        X = (lambda x, axes=(-2,-1): torch.fft.rfft2(x.float(), dim=axes))(x, axes=(-2, -1))
        W = w_r + 1j * w_i
        X = X * W
        y = (lambda x, s, axes=(-2,-1): torch.fft.irfft2(x, s=s, dim=axes).to(torch.bfloat16))(X, (S, D), axes=(-2, -1))
        return y

    a = _A_ref(A_u, A_log_A_real, A_A_imag, A_C_real, A_C_imag, A_log_dt, A_D_skip)
    b = _B_ref(B_x, B_w_r, B_w_i)
    return torch.concatenate([a, b], axis=-1)
```

```cpp
#include <hip/hip_runtime.h>
#include <hip/hip_bf16.h>
#include <stdint.h>

#define L_ 2048
#define D_ 1024
#define N_ 128
#define DF_ 513

__device__ __forceinline__ float bf2f(uint16_t v) { return __uint_as_float(((uint32_t)v) << 16); }
__device__ __forceinline__ uint16_t f2bf(float x) {
  uint32_t u = __float_as_uint(x);
  u += 0x7fffu + ((u >> 16) & 1u);
  return (uint16_t)(u >> 16);
}
__device__ __forceinline__ float rbf(float x) { return bf2f(f2bf(x)); }

__global__ void kgen(const uint16_t* logA, const uint16_t* Aim, const uint16_t* Cr, const uint16_t* Ci,
                     const uint16_t* logdt, float* K) {
  int d = blockIdx.y;
  int l = blockIdx.x * blockDim.x + threadIdx.x;
  float dt = rbf(expf(bf2f(logdt[d])));
  float pos = rbf((float)l);
  float acc = 0.f;
  for (int n = 0; n < N_; ++n) {
    float ar = -rbf(expf(bf2f(logA[d * N_ + n]))) * dt;
    float ai = bf2f(Aim[d * N_ + n]) * dt;
    float cr = bf2f(Cr[d * N_ + n]) * dt, ci = bf2f(Ci[d * N_ + n]) * dt;
    float e = expf(ar * pos);
    float s, c;
    sincosf(ai * pos, &s, &c);
    acc += cr * (e * c) - ci * (e * s);
  }
  K[d * L_ + l] = 2.f * acc;
}

__global__ void conv(const uint16_t* u, const float* K, const uint16_t* Dskip, uint16_t* out) {
  int d = blockIdx.x * blockDim.x + threadIdx.x;
  int l = blockIdx.y;
  float acc = 0.f;
  for (int j = 0; j <= l; ++j) acc += K[d * L_ + j] * bf2f(u[(l - j) * D_ + d]);
  float y = rbf(acc);
  float s = rbf(bf2f(u[l * D_ + d]) * bf2f(Dskip[d]));
  out[l * 2048 + d] = f2bf(y + s);
}

__global__ void rowdft(const uint16_t* x, float2* X1) {
  int k = blockIdx.x * blockDim.x + threadIdx.x;
  int s = blockIdx.y;
  if (k >= DF_) return;
  float re = 0, im = 0;
  for (int d = 0; d < D_; ++d) {
    float sn, cs;
    sincospif(-2.f * (float)((d * k) & 1023) / 1024.f, &sn, &cs);
    float v = bf2f(x[s * D_ + d]);
    re += v * cs; im += v * sn;
  }
  X1[s * DF_ + k] = make_float2(re, im);
}
__global__ void coldft(const float2* X1, const uint16_t* wr, const uint16_t* wi, float2* X2) {
  int k = blockIdx.x * blockDim.x + threadIdx.x;
  int t = blockIdx.y;
  if (k >= DF_) return;
  float re = 0, im = 0;
  for (int s = 0; s < L_; ++s) {
    float sn, cs;
    sincospif(-2.f * (float)((s * t) & 2047) / 2048.f, &sn, &cs);
    float2 v = X1[s * DF_ + k];
    re += v.x * cs - v.y * sn; im += v.x * sn + v.y * cs;
  }
  float a = bf2f(wr[t * DF_ + k]), b = bf2f(wi[t * DF_ + k]);
  X2[t * DF_ + k] = make_float2(re * a - im * b, re * b + im * a);
}
__global__ void colidft(const float2* X2, float2* X3) {
  int k = blockIdx.x * blockDim.x + threadIdx.x;
  int s = blockIdx.y;
  if (k >= DF_) return;
  float re = 0, im = 0;
  for (int t = 0; t < L_; ++t) {
    float sn, cs;
    sincospif(2.f * (float)((s * t) & 2047) / 2048.f, &sn, &cs);
    float2 v = X2[t * DF_ + k];
    re += v.x * cs - v.y * sn; im += v.x * sn + v.y * cs;
  }
  X3[s * DF_ + k] = make_float2(re / 2048.f, im / 2048.f);
}
__global__ void rowidft(const float2* X3, uint16_t* out) {
  int d = blockIdx.x * blockDim.x + threadIdx.x;
  int s = blockIdx.y;
  float acc = 0;
  for (int k = 1; k < 512; ++k) {
    float sn, cs;
    sincospif(2.f * (float)((d * k) & 1023) / 1024.f, &sn, &cs);
    float2 v = X3[s * DF_ + k];
    acc += v.x * cs - v.y * sn;
  }
  acc = 2.f * acc + X3[s * DF_].x + ((d & 1) ? -1.f : 1.f) * X3[s * DF_ + 512].x;
  out[s * 2048 + 1024 + d] = f2bf(acc / 1024.f);
}

extern "C" void kernel_launch(void* const* d_in, const int* in_sizes, int n_in,
                              void* d_out, int out_size, void* d_ws, size_t ws_size,
                              hipStream_t stream) {
  const uint16_t* u = (const uint16_t*)d_in[0];
  const uint16_t* logA = (const uint16_t*)d_in[1];
  const uint16_t* Aim = (const uint16_t*)d_in[2];
  const uint16_t* Cr = (const uint16_t*)d_in[3];
  const uint16_t* Ci = (const uint16_t*)d_in[4];
  const uint16_t* logdt = (const uint16_t*)d_in[5];
  const uint16_t* Dskip = (const uint16_t*)d_in[6];
  const uint16_t* x = (const uint16_t*)d_in[7];
  const uint16_t* wr = (const uint16_t*)d_in[8];
  const uint16_t* wi = (const uint16_t*)d_in[9];
  uint16_t* out = (uint16_t*)d_out;
  char* ws = (char*)d_ws;
  float* K = (float*)ws;
  float2* X1 = (float2*)(ws + (16 << 20));
  float2* X2 = (float2*)(ws + (32 << 20));
  float2* X3 = (float2*)(ws + (48 << 20));
  kgen<<<dim3(L_ / 256, D_), 256, 0, stream>>>(logA, Aim, Cr, Ci, logdt, K);
  conv<<<dim3(D_ / 256, L_), 256, 0, stream>>>(u, K, Dskip, out);
  rowdft<<<dim3(3, L_), 192, 0, stream>>>(x, X1);
  coldft<<<dim3(3, L_), 192, 0, stream>>>(X1, wr, wi, X2);
  colidft<<<dim3(3, L_), 192, 0, stream>>>(X2, X3);
  rowidft<<<dim3(D_ / 256, L_), 256, 0, stream>>>(X3, out);
}
```

```cpp
#include <hip/hip_runtime.h>
#include <hip/hip_bf16.h>
#include <stdint.h>

typedef __attribute__((ext_vector_type(8))) __bf16 bf16x8;
typedef __attribute__((ext_vector_type(2))) __bf16 bf16x2;
typedef __attribute__((ext_vector_type(2))) float f32x2;
typedef __attribute__((ext_vector_type(4))) float f32x4;
typedef __attribute__((ext_vector_type(4))) unsigned u32x4;
typedef __attribute__((ext_vector_type(2))) unsigned u32x2;
#define DI __device__ __forceinline__

#define STORE_Y 0
#define ROLE_MASK 3

#define MEMBAR() asm volatile("" ::: "memory")
#define PIN(x) asm volatile("" : "+v"(x))
#define PINF(f) do { PIN((f).re); PIN((f).im); } while (0)
#define SCHED() __builtin_amdgcn_sched_barrier(0)
#define DRAIN() asm volatile("s_waitcnt vmcnt(0)" ::: "memory")
#define AUX_NT 2
#define AUX_SC1 16
typedef __amdgpu_buffer_rsrc_t rsrc_t;
DI rsrc_t mkrsrc(const void* p, unsigned bytes) { return __builtin_amdgcn_make_buffer_rsrc((void*)p, 0, bytes, 0x00020000); }
#define LD32(r, off, aux) __builtin_amdgcn_raw_buffer_load_b32(r, off, 0, aux)
#define LD64(r, off, aux) __builtin_amdgcn_raw_buffer_load_b64(r, off, 0, aux)
#define LD128(r, off, aux) __builtin_amdgcn_raw_buffer_load_b128(r, off, 0, aux)
#define ST32(v, r, off, aux) __builtin_amdgcn_raw_buffer_store_b32(v, r, off, 0, aux)
#define ST64(v, r, off, aux) __builtin_amdgcn_raw_buffer_store_b64(v, r, off, 0, aux)
#define ST128(v, r, off, aux) __builtin_amdgcn_raw_buffer_store_b128(v, r, off, 0, aux)

DI unsigned pack2(float a, float b) { f32x2 v = {a, b}; bf16x2 r = __builtin_convertvector(v, bf16x2); return __builtin_bit_cast(unsigned, r); }
DI unsigned pack2v(f32x2 v) { bf16x2 r = __builtin_convertvector(v, bf16x2); return __builtin_bit_cast(unsigned, r); }
DI f32x4 mfma16(u32x4 a, u32x4 b, f32x4 c) {
  return __builtin_amdgcn_mfma_f32_16x16x32_bf16(__builtin_bit_cast(bf16x8, a), __builtin_bit_cast(bf16x8, b), c, 0, 0, 0);
}
DI float bflo(unsigned v) { return __uint_as_float(v << 16); }
DI float bfhi(unsigned v) { return __uint_as_float(v & 0xffff0000u); }
DI float rbf(float x) { return bflo(pack2(x, 0.f)); }
DI float cosr(float rev) { return __builtin_amdgcn_cosf(rev); }
DI float sinr(float rev) { return __builtin_amdgcn_sinf(rev); }
DI float mirror16(float v) { return __builtin_bit_cast(float, __builtin_amdgcn_update_dpp(0, __builtin_bit_cast(int, v), 0x140, 0xf, 0xf, true)); }
DI f32x4 mirror4(f32x4 v) { return f32x4{mirror16(v[0]), mirror16(v[1]), mirror16(v[2]), mirror16(v[3])}; }
DI u32x4 pack4(f32x4 re, f32x4 im) { return u32x4{pack2(re[0], im[0]), pack2(re[1], im[1]), pack2(re[2], im[2]), pack2(re[3], im[3])}; }
DI unsigned plo(unsigned a, unsigned b) { return __builtin_amdgcn_perm(b, a, 0x05040100u); }
DI unsigned phi(unsigned a, unsigned b) { return __builtin_amdgcn_perm(b, a, 0x07060302u); }
static constexpr f32x4 Z4 = {0.f, 0.f, 0.f, 0.f};

struct CFrag { u32x4 re, im; };
template <int SGN>
DI void setfrag(CFrag& f, int j, float rev) {
  float c = cosr(rev), s = sinr(rev);
  if (SGN > 0) { f.re[j] = pack2(c, s); f.im[j] = pack2(-s, c); }
  else { f.re[j] = pack2(c, -s); f.im[j] = pack2(s, c); }
}
DI CFrag conjfrag(const CFrag& f) {
  CFrag g;
#pragma unroll
  for (int j = 0; j < 4; ++j) { g.re[j] = f.re[j] ^ 0x80000000u; g.im[j] = f.im[j] ^ 0x00008000u; }
  return g;
}

#define WS_TW 0u
#define WS_T2 (8u << 20)
#define WS_UT (12u << 20)
#define WS_YT (16u << 20)
#define WS_XW (20u << 20)
#define WS_X2 ((20u << 20) + 65536u)
#define WS_F1 ((20u << 20) + 131072u)
#define WS_EP (WS_F1 + 4096u)
#define WS_FA (WS_F1 + 8192u)
#define WS_BYTES (21u << 20)
#define SENT 0xFFFFFFFFu
#define SPIN_CAP (1 << 18)
DI unsigned mktag(unsigned e) { return (e ^ 0x5bd1e995u) * 0x9E3779B1u + 0x7F4A7C15u; }
DI unsigned nosent(unsigned v) { return min(v, 0xFFFFFFFEu); }

#define XS 72
#define P3S (320 * 8)
#define L_X0 0
#define L_X1 18432
#define A_ST 0
#define A_STS 5712
#define A_UL (8 * A_STS)
#define A_ULS 4640
#define A_K (A_UL + 4 * A_ULS)
#define A_R2 1136
#define A_R3 1648
#define A_K1024 1968
#define LDS_BYTES (A_K + 4 * 2048)

struct Params {
  const uint16_t *u, *logA, *Aim, *Cr, *Ci, *logdt, *Dskip, *x, *wr, *wi;
  uint16_t* out;
  char* ws;
};

DI void role_spectral(const Params& P, char* lds, const int b) {
  char* ws = P.ws;
  const int lane = threadIdx.x & 63, w = __builtin_amdgcn_readfirstlane(threadIdx.x >> 6), idx = lane & 15, q = lane >> 4;

  const rsrc_t rws = mkrsrc(ws, WS_BYTES);
  const rsrc_t rx = mkrsrc(P.x, 2048 * 1024 * 2);
  const rsrc_t rwr = mkrsrc(P.wr, 2048 * 513 * 2);
  const rsrc_t rwi = mkrsrc(P.wi, 2048 * 513 * 2);
  const int n2 = b;
  const bool isq = idx >= 8;
  u32x2 v[4];
#pragma unroll
  for (int j = 0; j < 4; ++j) v[j] = LD64(rx, (256 * w + n2) * 2048 + (16 * (4 * q + j) + idx) * 8, AUX_NT);
  const unsigned ep = LD32(rws, WS_EP, AUX_SC1);
  u32x2 fr[2], fi[2];
  unsigned exr[2] = {0, 0}, exi[2] = {0, 0};
  const int ft = lane >> 2, fpart = lane & 3;
  const bool fq = fpart >= 2;
  {
    const int sp = 8 * n2 + w, sm = (2048 - sp) & 2047;
#pragma unroll
    for (int e = 0; e < 2; ++e) {
      const int k0 = 8 * ft + 256 * e;
      const int off = (fq ? sm * 513 + 505 - k0 : sp * 513 + k0) * 2 + (fpart & 1) * 8;
      fr[e] = LD64(rwr, off, AUX_NT);
      fi[e] = LD64(rwi, off, AUX_NT);
      const bool lo = lane == 61 || lane == 62, dc = e == 0 && (lane == 0 || lane == 3);
      if (lo || dc) {
        const int o2 = lo ? (fq ? off - 2 : off + 8) : (fq ? sp * 513 + 512 : sm * 513) * 2;
        exr[e] = __builtin_amdgcn_raw_buffer_load_b16(rwr, o2, 0, 0);
        exi[e] = __builtin_amdgcn_raw_buffer_load_b16(rwi, o2, 0, 0);
      }
    }
  }
  {
    const u32x4 s4 = {SENT, SENT, SENT, SENT};
#pragma unroll
    for (int j = 0; j < 2; ++j) ST128(s4, rws, WS_T2 + n2 * 16384 + (j * 512 + threadIdx.x) * 16, AUX_SC1);
    if (b < 2) ST128(s4, rws, WS_X2 + (b * 512 + threadIdx.x) * 16, AUX_SC1);
  }
  CFrag F;
  {
    CFrag M1, G;
    f32x4 twc[2], tws[2];
#pragma unroll
    for (int j = 0; j < 4; ++j) {
      setfrag<1>(F, j, (float)(((4 * q + j) * idx) & 15) * (1.f / 16));
      setfrag<1>(M1, j, (float)(((2 * (4 * q + j) + 1) * idx) & 31) * (1.f / 32));
      const int kk = idx & 7, e = idx >> 3, n1 = 2 * q + (j >> 1), c0 = j & 1;
      setfrag<1>(G, j, (float)((kk * (256 * n1 + n2) + 1024 * (c0 & e)) & 2047) * (1.f / 2048));
#pragma unroll
      for (int c = 0; c < 2; ++c) {
        float rev = (float)((2 * (4 * q + j) + c) * idx) * (1.f / 512);
        twc[c][j] = cosr(rev); tws[c][j] = sinr(rev);
      }
    }
    PINF(F); PINF(M1); PINF(G); PIN(twc[0]); PIN(twc[1]); PIN(tws[0]); PIN(tws[1]);
    SCHED();

    u32x4 o[2];
#pragma unroll
    for (int c = 0; c < 2; ++c) {
      u32x4 a = {v[0][c], v[1][c], v[2][c], v[3][c]};
      f32x4 dr = mfma16(a, F.re, Z4), di = mfma16(a, F.im, Z4);
      u32x4 bb = pack4(dr * twc[c] + di * tws[c], di * twc[c] - dr * tws[c]);
      o[c] = pack4(mfma16(c ? M1.re : F.re, bb, Z4), mfma16(c ? M1.im : F.im, bb, Z4));
    }
#pragma unroll
    for (int r = 0; r < 4; ++r) *(u32x2*)(lds + L_X0 + (idx + 16 * (4 * q + r)) * XS + w * 8) = u32x2{o[0][r], o[1][r]};

    __syncthreads();
#pragma unroll
    for (int tt = 0; tt < 3; ++tt) {
      if (tt == 2 && w != 0) break;
      const int t = tt == 2 ? 16 : 2 * w + tt;
      const int kap = 8 * t + (isq ? 15 - idx : idx);
      const int col = isq ? ((256 - kap) & 255) : kap;
      u32x2 lo = *(const u32x2*)(lds + L_X0 + col * XS + q * 16);
      u32x2 hi = *(const u32x2*)(lds + L_X0 + col * XS + q * 16 + 8);
      const unsigned m0 = isq ? 0x80000000u : 0u;
      const unsigned m1 = isq ? (kap == 0 ? 0x80000000u : 0x00008000u) : 0u;
      u32x4 d = {lo[0] ^ m0, lo[1] ^ m1, hi[0] ^ m0, hi[1] ^ m1};
      if (tt == 2) {
        f32x4 cr = mfma16(d, G.re, Z4), ci = mfma16(d, G.im, Z4);
        if (q == 0) ST32(pack2(cr[0], ci[0]), rws, WS_XW + (idx * 256 + n2) * 16, AUX_SC1);
        if (q == 3) ST32(pack2(cr[3], ci[3]), rws, WS_XW + (idx * 256 + n2) * 16 + 4, AUX_SC1);
      } else if (STORE_Y) {
        ST128(pack4(mfma16(d, G.re, Z4), mfma16(d, G.im, Z4)), rws, WS_TW + ((16 * t + idx) * 256 + n2) * 128 + 16 * q, AUX_SC1);
      } else {
        u32x4 s = pack4(mfma16(G.re, d, Z4), mfma16(G.im, d, Z4));
#pragma unroll
        for (int r = 0; r < 4; ++r) ST32(s[r], rws, WS_TW + ((16 * t + 4 * q + r) * 256 + n2) * 128 + idx * 4, AUX_SC1);
      }
    }
  }

#pragma unroll
  for (int e = 0; e < 2; ++e) {
    const unsigned sg = fq ? 0x80000000u : 0u;
    u32x4 o = {plo(fr[e][0], fi[e][0]), phi(fr[e][0], fi[e][0]), plo(fr[e][1], fi[e][1]), phi(fr[e][1], fi[e][1])};
    if (e == 0) {
      if (lane == 0) o[0] = pack2(0.5f * (bflo(o[0]) + bflo(exr[0])), 0.5f * (bfhi(o[0]) - bflo(exi[0])));
      if (lane == 3) o[3] = pack2(0.5f * (bflo(o[3]) + bflo(exr[0])), 0.5f * (bfhi(o[3]) - bflo(exi[0])));
    }
    o ^= u32x4{sg, sg, sg, sg};
    ST128(o, rws, WS_TW + ((w + 8 * e + 16 * ft) * 256 + n2) * 128 + 64 + fpart * 16, AUX_SC1);
    if (lane == 61 || lane == 62) ST32((exr[e] | (exi[e] << 16)) ^ sg, rws, WS_XW + ((w + 8 * e) * 256 + n2) * 16 + (fq ? 12 : 8), AUX_SC1);
  }
  const unsigned tag = mktag(__builtin_amdgcn_readfirstlane(ep));

  DRAIN();
  __builtin_amdgcn_s_barrier();
  if (threadIdx.x == 0) ST32(tag, rws, WS_F1 + (((n2 & 15) >> 1) * 32 + (n2 >> 4) * 2 + (n2 & 1)) * 4, AUX_SC1);

  const int k0 = 8 * (b >> 4) + 256 * ((b >> 3) & 1);
  CFrag A2[2], A3[2];
#pragma unroll
  for (int j = 0; j < 4; ++j)
#pragma unroll
    for (int jj = 0; jj < 2; ++jj) {
      const int j1 = 2 * w + jj;
      setfrag<1>(A2[jj], j, (float)(((4 * q + j) * (j1 + 16 * idx)) & 255) * (1.f / 256));
      setfrag<-1>(A3[jj], j, (float)((idx * (j1 + 16 * (4 * q + j))) & 255) * (1.f / 256));
    }
  const float nrm = 1.f / (2048.f * 512.f);
  float fs, fc;
  {
    const float psi = 0.125f - (float)(k0 + (isq ? 15 - idx : idx)) * (1.f / 2048);
    fs = sinr(psi); fc = cosr(psi);
  }
  const bool lo_wave = b >= 240 && w == 7;
#pragma unroll
  for (int jj = 0; jj < 2; ++jj) { PINF(A2[jj]); PINF(A3[jj]); }
  PIN(fs); PIN(fc);

  {
    int spins = 0;
    if (lo_wave) {
      for (;;) {
        MEMBAR();
        u32x4 f = LD128(rws, WS_F1 + lane * 16, AUX_SC1);
        bool ok = f[0] == tag && f[1] == tag && f[2] == tag && f[3] == tag;
        if (__all(ok) || ++spins > SPIN_CAP) break;
        __builtin_amdgcn_s_sleep(1);
      }
    } else {
      for (;;) {
        MEMBAR();
        unsigned f = LD32(rws, WS_F1 + w * 128 + (lane & 31) * 4, AUX_SC1);
        if (__all(f == tag) || ++spins > SPIN_CAP) break;
        __builtin_amdgcn_s_sleep(1);
      }
    }
    MEMBAR();
  }

  u32x4 xw[4];
  u32x4 wv[2];
  {
    u32x4 d[2];
#pragma unroll
    for (int mm = 0; mm < 2; ++mm)
#pragma unroll
      for (int j = 0; j < 4; ++j) d[mm][j] = LD32(rws, WS_TW + (b * 256 + 16 * (4 * q + j) + 2 * w + mm) * 128 + idx * 4, AUX_SC1);
#pragma unroll
    for (int mm = 0; mm < 2; ++mm)
#pragma unroll
      for (int j = 0; j < 4; ++j) wv[mm][j] = LD32(rws, WS_TW + (b * 256 + 16 * (4 * q + j) + 2 * w + mm) * 128 + 64 + idx * 4, AUX_SC1);
    if (lo_wave) {
#pragma unroll
      for (int j = 0; j < 4; ++j) xw[j] = LD128(rws, WS_XW + ((b & 15) * 256 + 16 * (4 * q + j) + idx) * 16, AUX_SC1);
    }
    u32x4 o[2];
#pragma unroll
    for (int mm = 0; mm < 2; ++mm) o[mm] = pack4(mfma16(F.re, d[mm], Z4), mfma16(F.im, d[mm], Z4));
#pragma unroll
    for (int r = 0; r < 4; ++r) *(u32x2*)(lds + L_X1 + ((4 * q + r) * 16 + idx) * XS + w * 8) = u32x2{o[0][r], o[1][r]};
  }

  __syncthreads();
  if (b == 0 && threadIdx.x == 0) ST32(ep + 1, rws, WS_EP, AUX_SC1);
  {
    u32x4 o[2];
    const float ns = fs * nrm, nc = fc * nrm;
#pragma unroll
    for (int jj = 0; jj < 2; ++jj) {
      const char* p = lds + L_X1 + ((2 * w + jj) * 16 + idx) * XS + q * 16;
      u32x2 lo = *(const u32x2*)p, hi = *(const u32x2*)(p + 8);
      u32x4 vv = {lo[0], lo[1], hi[0], hi[1]};
      f32x4 ar = mfma16(A2[jj].re, vv, Z4), ai = mfma16(A2[jj].im, vv, Z4);
      f32x4 gr = ar * fs - mirror4(ai) * fc, gi = ai * fs + mirror4(ar) * fc;
      f32x4 wr4 = {bflo(wv[jj][0]), bflo(wv[jj][1]), bflo(wv[jj][2]), bflo(wv[jj][3])};
      f32x4 wi4 = {bfhi(wv[jj][0]), bfhi(wv[jj][1]), bfhi(wv[jj][2]), bfhi(wv[jj][3])};
      f32x4 pr = wr4 * gr - wi4 * gi, pi = wr4 * gi + wi4 * gr;
      u32x4 y = pack4(pr * ns + mirror4(pi) * nc, pi * ns - mirror4(pr) * nc);
      o[jj] = pack4(mfma16(A3[jj].re, y, Z4), mfma16(A3[jj].im, y, Z4));
    }
#pragma unroll
    for (int r = 0; r < 4; ++r) *(u32x2*)(lds + L_X0 + ((4 * q + r) * 16 + idx) * XS + w * 8) = u32x2{o[0][r], o[1][r]};
  }
  __syncthreads();
  const CFrag Fc = conjfrag(F);
#pragma unroll
  for (int mm = 0; mm < 2; ++mm) {
    const int ma = 2 * w + mm;
    const char* p = lds + L_X0 + (ma * 16 + idx) * XS + q * 16;
    u32x2 lo = *(const u32x2*)p, hi = *(const u32x2*)(p + 8);
    u32x4 vv = {lo[0], lo[1], hi[0], hi[1]};
    if (STORE_Y) {
      u32x4 s = pack4(mfma16(vv, Fc.re, Z4), mfma16(vv, Fc.im, Z4));
#pragma unroll
      for (int r = 0; r < 4; ++r) s[r] = nosent(s[r]);
      ST128(s, rws, WS_T2 + ((ma + 16 * idx) * 256 + b) * 64 + 16 * q, AUX_SC1);
    } else {
      u32x4 s = pack4(mfma16(Fc.re, vv, Z4), mfma16(Fc.im, vv, Z4));
#pragma unroll
      for (int r = 0; r < 4; ++r) ST32(nosent(s[r]), rws, WS_T2 + ((ma + 16 * (4 * q + r)) * 256 + b) * 64 + idx * 4, AUX_SC1);
    }
  }
  if (lo_wave) {
    f32x4 tc, ts;
#pragma unroll
    for (int r = 0; r < 4; ++r) { float rev = (float)((4 * q + r) * idx) * (1.f / 256); tc[r] = cosr(rev); ts[r] = sinr(rev); }
    f32x4 sr[2], si[2];
#pragma unroll
    for (int c = 0; c < 2; ++c) {
      u32x4 a = {xw[0][c], xw[1][c], xw[2][c], xw[3][c]};
      f32x4 dr = mfma16(a, F.re, Z4), di = mfma16(a, F.im, Z4);
      u32x4 bb = pack4(dr * tc + di * ts, di * tc - dr * ts);
      sr[c] = mfma16(F.re, bb, Z4); si[c] = mfma16(F.im, bb, Z4);
    }
    const float psi = 0.125f - (float)(k0 + 8) * (1.f / 2048);
    const float s_ = sinr(psi), c_ = cosr(psi);
    f32x4 pr[2], pi[2];
#pragma unroll
    for (int c = 0; c < 2; ++c) {
      f32x4 gr = sr[c] * s_ - si[1 - c] * c_, gi = si[c] * s_ + sr[1 - c] * c_;
      f32x4 wr4 = {bflo(xw[0][2 + c]), bflo(xw[1][2 + c]), bflo(xw[2][2 + c]), bflo(xw[3][2 + c])};
      f32x4 wi4 = {bfhi(xw[0][2 + c]), bfhi(xw[1][2 + c]), bfhi(xw[2][2 + c]), bfhi(xw[3][2 + c])};
      pr[c] = wr4 * gr - wi4 * gi; pi[c] = wr4 * gi + wi4 * gr;
    }
    u32x4 y = pack4((pr[0] * s_ + pi[1] * c_) * nrm, (pi[0] * s_ - pr[1] * c_) * nrm);
    f32x4 dr = mfma16(y, Fc.re, Z4), di = mfma16(y, Fc.im, Z4);
    u32x4 bb = pack4(dr * tc - di * ts, di * tc + dr * ts);
    u32x4 s = pack4(mfma16(Fc.re, bb, Z4), mfma16(Fc.im, bb, Z4));
#pragma unroll
    for (int r = 0; r < 4; ++r) ST32(nosent(s[r]), rws, WS_X2 + ((b & 15) * 256 + idx + 16 * (4 * q + r)) * 4, AUX_SC1);
  }

  {
    int idx3 = idx, q3 = q;
    PIN(idx3); PIN(q3);
    CFrag M1c, Gc;
    f32x4 twc[2], tws[2];
#pragma unroll
    for (int j = 0; j < 4; ++j) {
      setfrag<-1>(M1c, j, (float)(((2 * idx3 + 1) * (4 * q3 + j)) & 31) * (1.f / 32));
      const int rho = 4 * q3 + j, kk = rho & 7, e = rho >> 3, n1 = idx3 >> 1, c0 = idx3 & 1;
      setfrag<-1>(Gc, j, (float)((kk * (256 * n1 + n2) + 1024 * (c0 & e)) & 2047) * (1.f / 2048));
#pragma unroll
      for (int c = 0; c < 2; ++c) {
        float rev = (float)((2 * idx3 + c) * (4 * q3 + j)) * (1.f / 512);
        twc[c][j] = cosr(rev); tws[c][j] = sinr(rev);
      }
    }
    PINF(M1c); PINF(Gc); PIN(twc[0]); PIN(twc[1]); PIN(tws[0]); PIN(tws[1]);
    const bool special0 = (w == 0 && idx == 15);
    u32x4 d[2];
    int spins = 0;

    for (;;) {
      MEMBAR();
      unsigned mx = 0;
#pragma unroll
      for (int tt = 0; tt < 2; ++tt)
#pragma unroll
        for (int j = 0; j < 4; ++j) {
          const int t = 2 * w + tt, rho = 4 * q + j;
          unsigned off = WS_T2 + (n2 * 256 + 16 * t + rho) * 64 + idx * 4;
          if (tt == 0 && special0) off = WS_X2 + (rho * 256 + n2) * 4;
          d[tt][j] = LD32(rws, off, AUX_SC1);
          mx = max(mx, d[tt][j]);
        }
      if (__all(mx != SENT) || ++spins > SPIN_CAP) break;
      __builtin_amdgcn_s_sleep(1);
    }

    char* l3 = lds + L_X1;
#pragma unroll
    for (int tt = 0; tt < 2; ++tt) {
      const int t = 2 * w + tt;
      u32x4 s = pack4(mfma16(Gc.re, d[tt], Z4), mfma16(Gc.im, d[tt], Z4));
      const bool special = tt == 0 && special0;
      const bool fl = isq && !special;
      int kap = 8 * t + (isq ? 15 - idx : idx);
      kap = special ? 128 : (isq ? 256 - kap : kap);
      const unsigned m0 = fl ? 0x80000000u : 0u, m1 = fl ? 0x00008000u : 0u;
      const int pos = (kap + 16 * (kap >> 6)) * 8;
#pragma unroll
      for (int h = 0; h < 2; ++h) *(u32x2*)(l3 + (2 * q + h) * P3S + pos) = u32x2{s[2 * h] ^ m0, s[2 * h + 1] ^ m1};
    }
    __syncthreads();
    u32x2 vv[4];
#pragma unroll
    for (int j = 0; j < 4; ++j) vv[j] = *(const u32x2*)(l3 + w * P3S + (idx + 16 * j + 80 * q) * 8);
    u32x4 o[2];
#pragma unroll
    for (int c = 0; c < 2; ++c) {
      u32x4 a = {vv[0][c], vv[1][c], vv[2][c], vv[3][c]};
      f32x4 dr = mfma16(a, c ? M1c.re : Fc.re, Z4), di = mfma16(a, c ? M1c.im : Fc.im, Z4);
      u32x4 bb = pack4(dr * twc[c] - di * tws[c], di * twc[c] + dr * tws[c]);
      o[c] = pack4(mfma16(Fc.re, bb, Z4), mfma16(Fc.im, bb, Z4));
    }
    unsigned* orow = (unsigned*)(P.out + (size_t)(256 * w + n2) * 2048 + 1024);
#pragma unroll
    for (int r = 0; r < 4; ++r) *(u32x2*)(orow + 2 * (16 * (4 * q + r) + idx)) = u32x2{o[0][r], o[1][r]};
  }

}

struct CZ { f32x2 z, s; };
DI CZ mkcz(f32x2 z) { return CZ{z, f32x2{-z.y, z.x}}; }
DI f32x2 cmul(f32x2 a, const CZ& c) { return __builtin_elementwise_fma(a.yy, c.s, a.xx * c.z); }
DI f32x2 cpow(float lr2, float rh, float rl, float e) {
  float m = __builtin_amdgcn_exp2f(lr2 * e);
  float t = rh * e;
  float er = __builtin_fmaf(rh, e, -t) + rl * e;
  float fr = __builtin_amdgcn_fractf(t) + er;
  return f32x2{m * cosr(fr), m * sinr(fr)};
}
DI u32x4 bandmask(int ph, int q, int lo, int hi, unsigned val) {
  unsigned bits = (((2u << (hi - lo)) - 1u) << (ph + lo)) >> (8 * q);
  u32x4 f;
#pragma unroll
  for (int j = 0; j < 4; ++j) {
    unsigned b0 = (unsigned)__builtin_amdgcn_sbfe((int)bits, 2 * j, 1), b1 = (unsigned)__builtin_amdgcn_sbfe((int)bits, 2 * j + 1, 1);
    f[j] = plo(b0, b1) & val;
  }
  return f;
}
template <int R0>
DI void strow4(unsigned base, unsigned a, unsigned b, unsigned c, unsigned d) {
  asm volatile("s_mov_b32 m0, %4\n\ts_nop 0\n\tds_write_addtid_b32 %0 offset:%5\n\tds_write_addtid_b32 %1 offset:%6\n\t"
               "ds_write_addtid_b32 %2 offset:%7\n\tds_write_addtid_b32 %3 offset:%8"
               :: "v"(a), "v"(b), "v"(c), "v"(d), "s"(base), "i"(R0 * 272), "i"((R0 + 1) * 272), "i"((R0 + 2) * 272), "i"((R0 + 3) * 272) : "memory");
}
template <int R0>
DI void strow1(unsigned base, unsigned a) {
  asm volatile("s_mov_b32 m0, %1\n\ts_nop 0\n\tds_write_addtid_b32 %0 offset:%2" :: "v"(a), "s"(base), "i"(R0 * 272) : "memory");
}
DI u32x4 hfrag(const unsigned* p, unsigned sh) {
  unsigned d0 = p[0], d1 = p[1], d2 = p[2], d3 = p[3], d4 = p[4];
  return u32x4{__builtin_amdgcn_alignbit(d1, d0, sh), __builtin_amdgcn_alignbit(d2, d1, sh),
               __builtin_amdgcn_alignbit(d3, d2, sh), __builtin_amdgcn_alignbit(d4, d3, sh)};
}
#define MSPLIT 13
#define PD 4

DI void role_ssm(const Params& P, char* lds, const int a) {
  char* ws = P.ws;
  const int grp = (a & 7) * 4 + ((a >> 3) & 3), mem = a >> 5, d0 = 32 * grp + 4 * mem;
  const int tid = threadIdx.x, lane = tid & 63, w = __builtin_amdgcn_readfirstlane(tid >> 6), idx = lane & 15, q = lane >> 4;
  const int ch = w & 3, hf = w >> 2;

  const rsrc_t rws = mkrsrc(ws, WS_BYTES);
  const rsrc_t ru = mkrsrc(P.u, 2048 * 1024 * 2);
  const int rq = lane >> 3, c8 = lane & 7;
  const int row0 = 256 * mem + 32 * w + 4 * rq;
  u32x2 uv[4];
#pragma unroll
  for (int r = 0; r < 4; ++r) uv[r] = LD64(ru, (row0 + r) * 2048 + grp * 64 + c8 * 8, AUX_NT);
  const int pn = (d0 + ch) * 128 + hf * 64 + lane;
  const unsigned pa = P.logA[pn], pi = P.Aim[pn], pr = P.Cr[pn], pc = P.Ci[pn], pd = P.logdt[d0 + ch], pk = P.Dskip[d0 + ch];
  const unsigned ep = LD32(rws, WS_FA + grp * 256 + 128, AUX_SC1);
  {
    const u32x4 s4 = {SENT, SENT, SENT, SENT};
#pragma unroll
    for (int j = 0; j < 2; ++j) ST128(s4, rws, WS_YT + d0 * 4096 + (j * 512 + tid) * 16, AUX_SC1);
  }
  *(u32x4*)(lds + A_K + tid * 16) = u32x4{0, 0, 0, 0};
  if (tid < 4 * 68) {
    const int c = tid / 68, o = tid % 68;
    *(u32x2*)(lds + A_UL + c * A_ULS + (o < 65 ? o * 8 : 4616 + (o - 65) * 8)) = u32x2{0, 0};
  }
  {
    const unsigned base = WS_UT + (32 * grp + 4 * c8) * 4096 + row0 * 2;
#pragma unroll
    for (int h = 0; h < 2; ++h) {
      ST64((u32x2{plo(uv[0][h], uv[1][h]), plo(uv[2][h], uv[3][h])}), rws, base + (2 * h) * 4096, AUX_SC1);
      ST64((u32x2{phi(uv[0][h], uv[1][h]), phi(uv[2][h], uv[3][h])}), rws, base + (2 * h + 1) * 4096, AUX_SC1);
    }
  }
  const unsigned tag = mktag(__builtin_amdgcn_readfirstlane(ep));

  char* st = lds + A_ST + w * A_STS;
  const unsigned stb = __builtin_amdgcn_readfirstlane((unsigned)(size_t)st);
  f32x4 D[2][2];
  {
    const float dt = rbf(expf(bflo(pd)));
    const float lr = -rbf(expf(bflo(pa))) * dt;
    const float th = bflo(pi) * dt;
    const float lr2 = lr * 1.44269504088896341f;
    const float ihi = 0.15915494309189535f, ilo = 6.4206382e-09f;
    const float rh = th * ihi, rl = __builtin_fmaf(th, ihi, -rh) + th * ilo;
    const f32x2 z8 = cpow(lr2, rh, rl, 8.f);
    const CZ c8z = mkcz(z8), c32 = mkcz(cpow(lr2, rh, rl, 32.f));
    const char* sr = st + idx * 272 + q * 16;
    u32x4 Bf[2][4];
    f32x2 c4[4];
    c4[0] = f32x2{1.f, 0.f}; c4[1] = z8; c4[2] = cmul(z8, c8z); c4[3] = cmul(c4[2], c8z);
#define BSTEP(S, MORE) do { strow4<4 * S>(stb, pack2v(c4[0]), pack2v(c4[1]), pack2v(c4[2]), pack2v(c4[3])); \
      if (MORE) { c4[0] = cmul(c4[0], c32); c4[1] = cmul(c4[1], c32); c4[2] = cmul(c4[2], c32); c4[3] = cmul(c4[3], c32); } } while (0)
    BSTEP(0, 1); BSTEP(1, 1); BSTEP(2, 1); BSTEP(3, 1);
#pragma unroll
    for (int ks = 0; ks < 4; ++ks) Bf[0][ks] = *(const u32x4*)(sr + ks * 64);
    BSTEP(0, 1); BSTEP(1, 1); BSTEP(2, 1); BSTEP(3, 0);
#pragma unroll
    for (int ks = 0; ks < 4; ++ks) Bf[1][ks] = *(const u32x4*)(sr + ks * 64);
    DRAIN();
    __builtin_amdgcn_s_barrier();
    if (tid == 0) ST32(tag, rws, WS_FA + grp * 256 + mem * 4, AUX_SC1);

    f32x2 z1 = cpow(lr2, rh, rl, 1.f), z256 = cpow(lr2, rh, rl, 256.f);
    z1.y = -z1.y; z256.y = -z256.y;
    const CZ c1 = mkcz(z1), c256 = mkcz(z256), c2 = mkcz(cmul(z1, c1));
    const f32x2 C = {2.f * bflo(pr) * dt, -2.f * bflo(pc) * dt};
    f32x2 a0 = C, a1 = cmul(C, c1), s0 = cmul(C, c256);
    f32x2 a2 = cmul(a0, c2), a3 = cmul(a1, c2), s1 = cmul(s0, c2);
    strow4<0>(stb, pack2v(a0), pack2v(a1), pack2v(a2), pack2v(a3));
    a0 = cmul(a2, c2); a1 = cmul(a3, c2); f32x2 s2 = cmul(s1, c2);
    a2 = cmul(a0, c2); a3 = cmul(a1, c2); f32x2 s3 = cmul(s2, c2);
    strow4<4>(stb, pack2v(a0), pack2v(a1), pack2v(a2), pack2v(a3));
    f32x2 s4 = cmul(s3, c2), t516 = cmul(s2, c256);
    strow4<8>(stb, pack2v(s0), pack2v(s1), pack2v(s2), pack2v(s3));
    f32x2 t520 = cmul(s4, c256), t772 = cmul(t516, c256);
    f32x2 t776 = cmul(t520, c256);
    strow4<12>(stb, pack2v(s4), pack2v(t520), pack2v(t516), pack2v(t776));
    const CZ c512 = mkcz(cmul(z256, c256));
    f32x2 e0 = cmul(t520, c512), e1 = cmul(t776, c512);
    f32x2 e2 = cmul(e0, c512), e3 = cmul(e1, c512);
    strow4<16>(stb, pack2v(t772), pack2v(e0), pack2v(e1), pack2v(e2));
    strow1<20>(stb, pack2v(e3));
    const int rw0 = idx < 8 ? 7 - idx : 8 + ((0x43222100u >> (4 * ((15 - idx) & 7))) & 7);
    const int rw1 = 13 + (idx & 7);
    const char* p0 = st + rw0 * 272 + q * 16;
    const char* p1 = st + rw1 * 272 + q * 16;
#pragma unroll
    for (int tb = 0; tb < 2; ++tb) { D[0][tb] = Z4; D[1][tb] = Z4; }
#pragma unroll
    for (int ks = 0; ks < 4; ++ks) {
      u32x4 f0 = *(const u32x4*)(p0 + ks * 64), f1 = *(const u32x4*)(p1 + ks * 64);
#pragma unroll
      for (int tb = 0; tb < 2; ++tb) { D[0][tb] = mfma16(f0, Bf[tb][ks], D[0][tb]); D[1][tb] = mfma16(f1, Bf[tb][ks], D[1][tb]); }
    }
  }
  *(f32x4*)(st + lane * 16) = hf ? D[0][0] : D[0][1];
  *(f32x4*)(st + 1024 + lane * 16) = hf ? D[1][0] : D[1][1];

  __syncthreads();
  char* kk = lds + A_K + ch * 2048;
  {
    const char* ps = lds + A_ST + (w ^ 4) * A_STS;
    f32x4 k0 = (hf ? D[0][1] : D[0][0]) + *(const f32x4*)(ps + lane * 16);
    f32x4 k1 = (hf ? D[1][1] : D[1][0]) + *(const f32x4*)(ps + 1024 + lane * 16);
    const int bc = 16 * hf + idx;
    const int x = 531 - 256 * (q >> 1) - 8 * bc - 7 + 4 * (q & 1);
    const unsigned w0 = pack2(k0[0], k0[1]);
    *(u32x2*)(kk + 2 * x) = u32x2{w0, pack2(k0[2], k0[3])};
    if (bc == 31 && q == 2) *(u32x2*)(kk + 2 * 16) = u32x2{w0 << 16, plo(w0, w0)};
    unsigned ka = pack2(k1[0], k1[1]), kc = pack2(k1[2], k1[3]);
    if (q == 0) {
      if (bc == 31) { *(unsigned*)(kk + A_K1024) = plo(kc, kc); kc &= 0xffff0000u; }
      *(unsigned*)(kk + A_R2 + 2 * (190 - 2 * bc)) = ka;
      *(unsigned*)(kk + A_R2 + 2 * (126 - 2 * bc)) = kc;
    }
    if (q == 1) {
      uint16_t* r3 = (uint16_t*)(kk + A_R3) + 127 - bc;
      r3[0] = (uint16_t)ka; r3[-32] = (uint16_t)(ka >> 16); r3[-64] = (uint16_t)kc; r3[-96] = (uint16_t)(kc >> 16);
    }
  }
  __syncthreads();

  const char* ul = lds + A_UL + ch * A_ULS;
  const char* ub = ul + 512 + 32 * idx + 16 * q;
  const int x0 = 527 - idx + 8 * q;
  const unsigned sh = (x0 & 1) * 16;
  const unsigned* hp = (const unsigned*)(kk + 4 * (x0 >> 1));
  f32x4 acc[8];
#pragma unroll
  for (int t = 0; t < 8; ++t) acc[t] = Z4;

  auto fetch_u = [&]() {

    int spins = 0;
    for (;;) {
      MEMBAR();
      unsigned f = LD32(rws, WS_FA + grp * 256 + (lane & 7) * 4, AUX_SC1);
      if (__all(f == tag) || ++spins > SPIN_CAP) break;
      __builtin_amdgcn_s_sleep(1);
    }
    MEMBAR();

    u32x4 g[2];
#pragma unroll
    for (int j = 0; j < 2; ++j) g[j] = LD128(rws, WS_UT + d0 * 4096 + (j * 512 + tid) * 16, AUX_SC1);
#pragma unroll
    for (int j = 0; j < 2; ++j) {
      char* dst = lds + A_UL + (2 * j + (tid >> 8)) * A_ULS + 520 + (tid & 255) * 16;
      *(u32x2*)dst = u32x2{g[j][0], g[j][1]};
      *(u32x2*)(dst + 8) = u32x2{g[j][2], g[j][3]};
    }
    __syncthreads();

  };

  if (hf == 0) {
    u32x4 H[MSPLIT];
#pragma unroll
    for (int m = 0; m < MSPLIT; ++m) { H[m] = hfrag(hp - 16 * m, sh); PIN(H[m]); }
    fetch_u();
    u32x4 ring[PD + 1];
#pragma unroll
    for (int i = 0; i < PD; ++i) ring[i] = *(const u32x4*)(ub + 64 * (i - 8));
#pragma unroll
    for (int f = -8; f <= 56; ++f) {
      if (f + PD <= 56) ring[(f + 8 + PD) % (PD + 1)] = *(const u32x4*)(ub + 64 * (f + PD));
#pragma unroll
      for (int al = 0; al < 8; ++al) {
        const int m = 8 * al - f;
        if (m >= 0 && m < MSPLIT) acc[al] = mfma16(H[m], ring[(f + 8) % (PD + 1)], acc[al]);
      }
      SCHED();
    }
  } else {
    u32x4 H[17 - MSPLIT], T2[6], T3[4];
#pragma unroll
    for (int m = MSPLIT; m < 17; ++m) { H[m - MSPLIT] = hfrag(hp - 16 * m, sh); PIN(H[m - MSPLIT]); }
#pragma unroll
    for (int dl = 3; dl <= 8; ++dl) {
      const char* p = kk + A_R2 + 2 * (316 - 32 * dl - 4 * idx + 8 * q);
      u32x2 lo = *(const u32x2*)p, hi = *(const u32x2*)(p + 8);
      T2[dl - 3] = u32x4{lo[0], lo[1], hi[0], hi[1]}; PIN(T2[dl - 3]);
    }
#pragma unroll
    for (int dl = 4; dl <= 7; ++dl) {
      const unsigned* p = (const unsigned*)(kk + A_R3 + 2 * (254 - 32 * dl - 2 * idx + 8 * q));
      T3[dl - 4] = u32x4{p[0], p[1], p[2], p[3]}; PIN(T3[dl - 4]);
    }
    u32x4 bx0 = bandmask(idx, q, 2, 6, 0x3f803f80u), bx1 = bandmask(idx, q, 7, 9, 0x3f803f80u);
    u32x4 by0 = bandmask(idx, q, 0, 8, 0x3f803f80u), by1 = bandmask(idx, q, 9, 15, 0x3f803f80u);
    u32x4 h32 = bandmask(idx, q, 0, 6, *(const unsigned*)(kk + A_K1024));
    PIN(bx0); PIN(bx1); PIN(by0); PIN(by1); PIN(h32);
    fetch_u();
    const char* wc = ul + 480 + 32 * idx + 16 * q;
#pragma unroll
    for (int ks = 0; ks < 4; ++ks) {
      u32x4 wn = *(const u32x4*)(wc + 512 * ks);
      u32x4 v = pack4(mfma16(wn, by0, Z4), mfma16(wn, by1, Z4));
#pragma unroll
      for (int al = ks + 4; al < 8; ++al) acc[al] = mfma16(v, T3[al - ks - 4], acc[al]);
    }
#pragma unroll
    for (int f = -8; f <= 56 - MSPLIT; ++f) {
      const int m = MSPLIT + ((8 * 16 - f - MSPLIT) & 7);
      if (m > 16) continue;
      u32x4 uf = *(const u32x4*)(ub + 64 * f);
      acc[(m + f) / 8] = mfma16(H[m - MSPLIT], uf, acc[(m + f) / 8]);
      if (m == 16 && f <= 24) acc[(f + 32) / 8] = mfma16(h32, uf, acc[(f + 32) / 8]);
    }
    const char* wb = ul + 480 + 16 * idx + 16 * q;
#pragma unroll
    for (int ks = 0; ks < 12; ++ks) {
      u32x4 wn = *(const u32x4*)(wb + 256 * ks);
      u32x4 v = pack4(mfma16(wn, bx0, Z4), mfma16(wn, bx1, Z4));
#pragma unroll
      for (int al = 2; al < 8; ++al) {
        const int dl = 2 * al - ks;
        if (dl >= 3 && dl <= 8) acc[al] = mfma16(v, T2[dl - 3], acc[al]);
      }
    }
  }

  if (mem == 0 && tid == 0) ST32(ep + 1, rws, WS_FA + grp * 256 + 128, AUX_SC1);
#pragma unroll
  for (int t = 0; t < 4; ++t) *(f32x4*)(st + t * 1024 + lane * 16) = hf ? acc[t] : acc[4 + t];
  __syncthreads();
  {
    const char* ps = lds + A_ST + (w ^ 4) * A_STS + lane * 16;
    const float ds = bflo(pk);
#pragma unroll
    for (int t = 0; t < 4; ++t) {
      f32x4 y = (hf ? acc[4 + t] : acc[t]) + *(const f32x4*)(ps + t * 1024);
      const int lb = 512 * (4 * hf + t) + 32 * idx + 8 * q;
      u32x2 uu = *(const u32x2*)(ul + 520 + lb);
      u32x2 o;
#pragma unroll
      for (int h = 0; h < 2; ++h) {
        unsigned yb = pack2(y[2 * h], y[2 * h + 1]);
        unsigned s = pack2(bflo(uu[h]) * ds, bfhi(uu[h]) * ds);
        o[h] = nosent(pack2(bflo(yb) + bflo(s), bfhi(yb) + bfhi(s)));
      }
      ST64(o, rws, WS_YT + (d0 + ch) * 4096 + lb, AUX_SC1);
    }
  }

  {
    u32x2 y[4];
    int spins = 0;
    const unsigned base = WS_YT + (32 * grp + 4 * c8) * 4096 + row0 * 2;
    for (;;) {
      MEMBAR();
      unsigned mx = 0;
#pragma unroll
      for (int c = 0; c < 4; ++c) { y[c] = LD64(rws, base + c * 4096, AUX_SC1); mx = max(mx, max(y[c][0], y[c][1])); }
      if (__all(mx != SENT) || ++spins > SPIN_CAP) break;
      __builtin_amdgcn_s_sleep(1);
    }

    uint16_t* op = P.out + (size_t)row0 * 2048 + 32 * grp + 4 * c8;
#pragma unroll
    for (int h = 0; h < 2; ++h) {
      *(u32x2*)(op + (2 * h) * 2048) = u32x2{plo(y[0][h], y[1][h]), plo(y[2][h], y[3][h])};
      *(u32x2*)(op + (2 * h + 1) * 2048) = u32x2{phi(y[0][h], y[1][h]), phi(y[2][h], y[3][h])};
    }
  }

}

__global__ __launch_bounds__(512, 4) void fused(Params P) {
  extern __shared__ __attribute__((aligned(16))) char lds[];
  const int b = blockIdx.x;
  if (b < 256) { if (ROLE_MASK & 1) role_spectral(P, lds, b); }
  else { if (ROLE_MASK & 2) role_ssm(P, lds, b - 256); }
}

extern "C" void kernel_launch(void* const* d_in, const int* in_sizes, int n_in,
                              void* d_out, int out_size, void* d_ws, size_t ws_size,
                              hipStream_t stream) {
  Params P;
  P.u = (const uint16_t*)d_in[0]; P.logA = (const uint16_t*)d_in[1]; P.Aim = (const uint16_t*)d_in[2]; P.Cr = (const uint16_t*)d_in[3];
  P.Ci = (const uint16_t*)d_in[4]; P.logdt = (const uint16_t*)d_in[5]; P.Dskip = (const uint16_t*)d_in[6]; P.x = (const uint16_t*)d_in[7];
  P.wr = (const uint16_t*)d_in[8]; P.wi = (const uint16_t*)d_in[9]; P.out = (uint16_t*)d_out; P.ws = (char*)d_ws;
  (void)hipFuncSetAttribute((const void*)fused, hipFuncAttributeMaxDynamicSharedMemorySize, LDS_BYTES);
  fused<<<512, 512, LDS_BYTES, stream>>>(P);
}
```

```cpp
#include <hip/hip_runtime.h>
#include <hip/hip_bf16.h>
#include <stdint.h>

typedef __attribute__((ext_vector_type(8))) __bf16 bf16x8;
typedef __attribute__((ext_vector_type(2))) __bf16 bf16x2;
typedef __attribute__((ext_vector_type(2))) float f32x2;
typedef __attribute__((ext_vector_type(4))) float f32x4;
typedef __attribute__((ext_vector_type(4))) unsigned u32x4;
typedef __attribute__((ext_vector_type(2))) unsigned u32x2;
typedef __attribute__((ext_vector_type(4))) short s16x4;
#define LAS __attribute__((address_space(3)))
#define DI __device__ __forceinline__

#define ROLE_MASK 3

#define MEMBAR() asm volatile("" ::: "memory")
#define PIN(x) asm volatile("" : "+v"(x))
#define PINF(f) do { PIN((f).re); PIN((f).im); } while (0)
#define SCHED() __builtin_amdgcn_sched_barrier(0)
#define DRAIN() asm volatile("s_waitcnt vmcnt(0)" ::: "memory")
#define AUX_NT 2
#define AUX_SC1 16
typedef __amdgpu_buffer_rsrc_t rsrc_t;
DI rsrc_t mkrsrc(const void* p, unsigned bytes) { return __builtin_amdgcn_make_buffer_rsrc((void*)p, 0, bytes, 0x00020000); }
#define LD64S(r, off, soff, aux) __builtin_amdgcn_raw_buffer_load_b64(r, off, soff, aux)
#define ST64S(v, r, off, soff, aux) __builtin_amdgcn_raw_buffer_store_b64(v, r, off, soff, aux)
#define LDSBAR() do { asm volatile("s_waitcnt lgkmcnt(0)" ::: "memory"); __builtin_amdgcn_s_barrier(); MEMBAR(); } while (0)
#define LD32(r, off, aux) __builtin_amdgcn_raw_buffer_load_b32(r, off, 0, aux)
#define LD64(r, off, aux) __builtin_amdgcn_raw_buffer_load_b64(r, off, 0, aux)
#define LD128(r, off, aux) __builtin_amdgcn_raw_buffer_load_b128(r, off, 0, aux)
#define ST32(v, r, off, aux) __builtin_amdgcn_raw_buffer_store_b32(v, r, off, 0, aux)
#define ST64(v, r, off, aux) __builtin_amdgcn_raw_buffer_store_b64(v, r, off, 0, aux)
#define ST128(v, r, off, aux) __builtin_amdgcn_raw_buffer_store_b128(v, r, off, 0, aux)

DI unsigned pack2(float a, float b) { f32x2 v = {a, b}; bf16x2 r = __builtin_convertvector(v, bf16x2); return __builtin_bit_cast(unsigned, r); }
DI unsigned pack2v(f32x2 v) { bf16x2 r = __builtin_convertvector(v, bf16x2); return __builtin_bit_cast(unsigned, r); }
DI f32x4 mfma16(u32x4 a, u32x4 b, f32x4 c) {
  return __builtin_amdgcn_mfma_f32_16x16x32_bf16(__builtin_bit_cast(bf16x8, a), __builtin_bit_cast(bf16x8, b), c, 0, 0, 0);
}
DI float bflo(unsigned v) { return __uint_as_float(v << 16); }
DI float bfhi(unsigned v) { return __uint_as_float(v & 0xffff0000u); }
DI float rbf(float x) { return bflo(pack2(x, 0.f)); }
DI float cosr(float rev) { return __builtin_amdgcn_cosf(rev); }
DI float sinr(float rev) { return __builtin_amdgcn_sinf(rev); }
DI float mirror16(float v) { return __builtin_bit_cast(float, __builtin_amdgcn_update_dpp(0, __builtin_bit_cast(int, v), 0x140, 0xf, 0xf, true)); }
DI f32x4 mirror4(f32x4 v) { return f32x4{mirror16(v[0]), mirror16(v[1]), mirror16(v[2]), mirror16(v[3])}; }
DI u32x4 pack4(f32x4 re, f32x4 im) { return u32x4{pack2(re[0], im[0]), pack2(re[1], im[1]), pack2(re[2], im[2]), pack2(re[3], im[3])}; }
DI unsigned plo(unsigned a, unsigned b) { return __builtin_amdgcn_perm(b, a, 0x05040100u); }
DI unsigned phi(unsigned a, unsigned b) { return __builtin_amdgcn_perm(b, a, 0x07060302u); }
static constexpr f32x4 Z4 = {0.f, 0.f, 0.f, 0.f};

struct CFrag { u32x4 re, im; };
template <int SGN>
DI void setfrag(CFrag& f, int j, float rev) {
  float c = cosr(rev), s = sinr(rev);
  if (SGN > 0) { f.re[j] = pack2(c, s); f.im[j] = pack2(-s, c); }
  else { f.re[j] = pack2(c, -s); f.im[j] = pack2(s, c); }
}
DI CFrag conjfrag(const CFrag& f) {
  CFrag g;
#pragma unroll
  for (int j = 0; j < 4; ++j) { g.re[j] = f.re[j] ^ 0x80000000u; g.im[j] = f.im[j] ^ 0x00008000u; }
  return g;
}

#define WS_T 0u
#define WS_F1 (4u << 20)
#define WS_EP (WS_F1 + 786432u)
#define WS_BYTES (5u << 20)
#define SETUP_FIRST 0
#define USE_PRIO 1
#define PRIO(n) do { if (USE_PRIO) __builtin_amdgcn_s_setprio(n); } while (0)
#define W_POS 1
#define T_SWZ 1
#define POLL_MODE 0
#define NREP 32
#define REP_STRIDE 512
#define POLL_SLEEP 1
#define SPIN_CAP (1 << 18)
#define OUT_AUX 16
DI unsigned mktag(unsigned e) { return (e ^ 0x5bd1e995u) * 0x9E3779B1u + 0x7F4A7C15u; }

#define XS 72
#define ES 2560
#define L_R0 0
#define L_R1 40960
#define L_W 81920
#define WRS 1056
#define W_EX (32 * WRS)
#define L_C (L_W + 33920)
#define A_STS 4352
#define A_UL (16 * A_STS)
#define A_ULS 4640
#define A_K (A_UL + 8 * A_ULS)
#define A_R2 1136
#define A_R3 1648
#define A_K1024 1968
#define A_M (A_K + 8 * 2048)
#define A_PT 0
#define A_Y 32768
#define A_YS 1056
#define LDS_BYTES (L_C + 16384)

struct Params {
  const uint16_t *u, *logA, *Aim, *Cr, *Ci, *logdt, *Dskip, *x, *wr, *wi;
  uint16_t* out;
  char* ws;
};
DI u32x4 xor4(u32x4 a, unsigned m) { return a ^ u32x4{m, m, m, m}; }
DI unsigned mirru(unsigned v) { return (unsigned)__builtin_amdgcn_update_dpp(0, (int)v, 0x140, 0xf, 0xf, true); }
DI f32x4 unpk4(u32x2 v) { return f32x4{bflo(v[0]), bfhi(v[0]), bflo(v[1]), bfhi(v[1])}; }
DI u32x2 trread(const char* p) { return __builtin_bit_cast(u32x2, __builtin_amdgcn_ds_read_tr16_b64_v4i16((LAS s16x4*)p)); }
DI CFrag ldfrag(const char* p) { CFrag f; f.re = *(const u32x4*)p; f.im = *(const u32x4*)(p + 1024); return f; }

enum { C_F = 0, C_M1, C_TW0, C_TW1, C_G, C_M1C, C_TW1C, C_GC };
struct WSet { f32x4 r[2], i[2]; };

DI float ldbf(const char* p) { return bflo(*(const uint16_t*)p); }
DI WSet read_filter(const char* const* p1, const char* const* p2, bool first, bool dc) {
  WSet s;
#pragma unroll
  for (int r = 0; r < 4; ++r) {
    s.r[0][r] = ldbf(p1[r]); s.i[0][r] = ldbf(p1[r] + 16 * WRS);
    s.r[1][r] = ldbf(p2[r]); s.i[1][r] = ldbf(p2[r] + 16 * WRS);
  }
  if (first) {
    const float h = dc ? 0.5f : 1.f, g = dc ? 0.5f : 0.f;
    const int o = dc ? 1024 : 0;
#pragma unroll
    for (int r = 0; r < 4; ++r) {
      s.r[0][r] = s.r[0][r] * h + ldbf(p2[r] - o) * g; s.i[0][r] = s.i[0][r] * h - ldbf(p2[r] - o + 16 * WRS) * g;
      s.r[1][r] = s.r[1][r] * h + ldbf(p1[r] + o) * g; s.i[1][r] = s.i[1][r] * h - ldbf(p1[r] + o + 16 * WRS) * g;
    }
  }
  return s;
}
struct Coef { f32x4 ar, ai, gr, gi, br, bi; };
DI Coef mkcoef(const WSet& s, float ss, float cc, float sc) {
  Coef c;
  c.ar = s.r[0] * ss + s.r[1] * cc; c.ai = s.i[0] * ss - s.i[1] * cc;
  c.gr = s.r[0] * cc + s.r[1] * ss; c.gi = s.i[0] * cc - s.i[1] * ss;
  c.br = (s.i[1] + s.i[0]) * -sc; c.bi = (s.r[0] - s.r[1]) * sc;
  return c;
}
struct WRaw { u32x4 r, i, e; };
DI void role_spectral(const Params& P, char* lds, const int b) {
  char* ws = P.ws;
  const int lane = threadIdx.x & 63, w = __builtin_amdgcn_readfirstlane(threadIdx.x >> 6), idx = lane & 15, q = lane >> 4;
  const int hb = w >> 3, w8 = w & 7;

  const rsrc_t rws = mkrsrc(ws, WS_BYTES);
  const rsrc_t rx = mkrsrc(P.x, 2048 * 1024 * 2);
  const rsrc_t rwr = mkrsrc(P.wr, 2048 * 513 * 2);
  const rsrc_t rwi = mkrsrc(P.wi, 2048 * 513 * 2);
  const int n2 = b & 7, cg = b >> 3;
  const bool special = b == 0;
  const unsigned xoff = ((64 * q + w) * 8 + n2) * 2048 + cg * 128 + idx * 8;
  unsigned toff = xoff ^ (T_SWZ * w * 128);
  u32x2 v[4];
#pragma unroll
  for (int j = 0; j < 4; ++j) v[j] = LD64S(rx, xoff, j * 262144, AUX_NT);
  const unsigned ep = LD32(rws, WS_EP, AUX_SC1);
  char* cl = lds + L_C + lane * 16;
  CFrag F;
#pragma unroll
  for (int j = 0; j < 4; ++j) setfrag<1>(F, j, (float)(((4 * q + j) * idx) & 15) * (1.f / 16));
  int ewr = (4 * q) * ES + (w + (w >> 2)) * 128 + idx * 8;
  int erd = w * ES + (5 * q) * 128 + idx * 8;
  CFrag A2;
  {
    const int k1 = w + 16 * idx;
    const int tn = (k1 > 128 ? k1 - 256 : k1) * n2;
#pragma unroll
    for (int j = 0; j < 4; ++j) setfrag<1>(A2, j, (float)((8 * (4 * q + j) * k1 + tn) & 2047) * (1.f / 2048));
    A2.im = xor4(A2.im, k1 > 128 ? 0x80008000u : 0u);
  }
  PINF(A2); PIN(ewr); PIN(erd);
  WRaw wraw;
  auto load_filter = [&]() {
    const int row = special ? 128 * hb + 256 * w8 : (hb ? 2048 - b - 256 * w8 : b + 256 * w8);
    const int off = (row * 1026) & ~15;
    wraw.r = __builtin_amdgcn_raw_buffer_load_b128(rwr, lane * 16, off, AUX_NT);
    wraw.i = __builtin_amdgcn_raw_buffer_load_b128(rwi, lane * 16, off, AUX_NT);
    const uint16_t* tail = (lane & 1) ? P.wi : P.wr;
    wraw.e = *(const u32x4*)((const char*)tail + off + 1024);
  };
  MEMBAR();
  if (W_POS == 0) load_filter();
  {
    PINF(F);
    SCHED();

    PRIO(3);
    u32x4 o[2];
#pragma unroll
    for (int tt = 0; tt < 2; ++tt) {
      u32x4 dd = {v[0][tt], v[1][tt], v[2][tt], v[3][tt]};
      o[tt] = pack4(mfma16(F.re, dd, Z4), mfma16(F.im, dd, Z4));
    }
#pragma unroll
    for (int r = 0; r < 4; ++r) *(u32x2*)(lds + L_R0 + ewr + r * ES) = u32x2{o[0][r], o[1][r]};

    MEMBAR();
    if (W_POS == 1) load_filter();
    LDSBAR();
    u32x2 e[4];
#pragma unroll
    for (int j = 0; j < 4; ++j) e[j] = *(const u32x2*)(lds + L_R0 + erd + j * 128);
#pragma unroll
    for (int tt = 0; tt < 2; ++tt) {
      u32x4 vv = {e[0][tt], e[1][tt], e[2][tt], e[3][tt]};
      o[tt] = pack4(mfma16(A2.re, vv, Z4), mfma16(A2.im, vv, Z4));
    }
#pragma unroll
    for (int r = 0; r < 4; ++r) ST64S((u32x2{o[0][r] & ~1u, o[1][r]}), rws, toff, r * 262144, AUX_SC1);
  }

  PRIO(0);
  const unsigned tag = mktag(__builtin_amdgcn_readfirstlane(ep));
  MEMBAR();
  if (w >= 1 && w < 8) {
    const int e = (0x83783377 >> (4 * w)) & 15, h = (0x90 >> w) & 1, c = ((0x6A >> w) & 1) << (e - 1);
    const bool swap = w >= C_M1C, tw = (0x4C >> w) & 1;
    const float unit = ((0xA0 >> w) & 1) ? -1.f / 2048 : 1.f / 2048;
    u32x4 c0, c1;
#pragma unroll
    for (int j = 0; j < 4; ++j) {
      const int s = 4 * q + j, x = swap ? idx : s, y = swap ? s : idx;
      const int m = (((x >> h) << e) + c) * y + ((h & x & (y >> 3)) << 10);
      const float rev = (float)(m & 2047) * unit;
      const float cs = cosr(rev), sn = sinr(rev);
      unsigned p0 = pack2(cs, sn), p1 = pack2(-sn, cs);
      PIN(p0); PIN(p1);
      c0[j] = tw ? __float_as_uint(cs) : p0;
      c1[j] = tw ? __float_as_uint(sn) : p1;
    }
    *(u32x4*)(cl + w * 2048) = c0;
    *(u32x4*)(cl + w * 2048 + 1024) = c1;
  }
  float ss, cc, sc;
  {
    const float nrm = 1.f / (2048.f * 512.f);
    const float th = (float)(16 * w + idx + 256 * (q >> 1)) * (1.f / 1024);
    const float sn = sinr(th) * (0.5f * nrm);
    ss = 0.5f * nrm - sn; cc = 0.5f * nrm + sn; sc = cosr(th) * (0.5f * nrm);
  }
  {
    char* wl = lds + L_W + w * WRS;
    *(u32x4*)(wl + lane * 16) = wraw.r;
    *(u32x4*)(wl + 16 * WRS + lane * 16) = wraw.i;
    if (lane < 2) *(u32x4*)(wl + lane * (16 * WRS) + 1024) = wraw.e;
  }
  auto publish = [&]() {
    asm volatile("s_waitcnt vmcnt(0) lgkmcnt(0)" ::: "memory");
    __builtin_amdgcn_s_barrier();
    MEMBAR();
    if (threadIdx.x < NREP) ST32(tag, rws, WS_F1 + threadIdx.x * REP_STRIDE + (n2 * 16 + cg) * 4, AUX_SC1);

  };
  if (SETUP_FIRST) LDSBAR(); else publish();
  CFrag M1 = ldfrag(cl + C_M1 * 2048), G = ldfrag(cl + C_G * 2048);
  f32x4 twc[3], tws[3];
#pragma unroll
  for (int c = 0; c < 2; ++c) { twc[c] = *(const f32x4*)(cl + (C_TW0 + c) * 2048); tws[c] = *(const f32x4*)(cl + (C_TW0 + c) * 2048 + 1024); }
  Coef cf;
  {
    const int k = 16 * w + idx + 256 * (q >> 1);
    const bool dc = k == 0;
    const char* p1[4]; const char* p2[4];
    const char* b1 = lds + L_W + 2 * k, * b2 = lds + L_W + 1024 - 2 * k;
    if (!special) {
#pragma unroll
      for (int r = 0; r < 4; ++r) { p1[r] = b1 + (4 * (q & 1) + r) * WRS + 2 * (b & 7); p2[r] = b2 + (8 + 4 * (q & 1) + r) * WRS + 2 * (-b & 7); }
      cf = mkcoef(read_filter(p1, p2, w == 0, dc), ss, cc, sc);
    } else {
#pragma unroll
      for (int r = 0; r < 4; ++r) { p1[r] = b1 + (4 * (q & 1) + r) * WRS; p2[r] = b2 + ((8 - 4 * (q & 1) - r) & 7) * WRS; }
      cf = mkcoef(read_filter(p1, p2, w == 0, dc), ss, cc, sc);
#pragma unroll
      for (int r = 0; r < 4; ++r) { p1[r] = b1 + (8 + 4 * (q & 1) + r) * WRS; p2[r] = b2 + (15 - 4 * (q & 1) - r) * WRS; }
      const Coef c2 = mkcoef(read_filter(p1, p2, w == 0, dc), ss, cc, sc);
      cf.gr = c2.ar; cf.gi = c2.ai;
      *(f32x4*)(lds + L_R0 + threadIdx.x * 16) = c2.br;
      *(f32x4*)(lds + L_R0 + 16384 + threadIdx.x * 16) = c2.bi;
    }
  }
  PIN(cf.ar); PIN(cf.ai); PIN(cf.gr); PIN(cf.gi); PIN(cf.br); PIN(cf.bi);
  if (SETUP_FIRST) publish();
  const int krow = special ? 128 * hb : (hb ? 256 - b : b);
  unsigned poff = ((krow * 8 + w8) * 2048 + (64 * q + idx) * 8) ^ (T_SWZ * (krow & 12) * 128);
  const int pj = T_SWZ * (krow & 3);
  const int kp = 16 * w + idx;
  int wa = hb * (256 * XS) + (idx + 64 * q) * XS + w8 * 8;
  int rp = kp * XS + q * 16, pp = (2 * q) * ES + (kp + 16 * (kp >> 6)) * 8;
  int rd = w * ES + (idx + 80 * q) * 8;
  PINF(M1); PINF(G); PIN(twc[0]); PIN(twc[1]); PIN(tws[0]); PIN(tws[1]);
  PIN(wa); PIN(poff); PIN(rp); PIN(pp); PIN(rd);
  PIN(cf.ar); PIN(cf.ai); PIN(cf.gr); PIN(cf.gi); PIN(cf.br); PIN(cf.bi);

  {
    int spins = 0;
    if (POLL_MODE == 0) {
      for (;;) {
        MEMBAR();
        unsigned f = LD32(rws, WS_F1 + (b % NREP) * REP_STRIDE + (w8 * 16 + idx) * 4, AUX_SC1);
        if (__all(f == tag) || ++spins > SPIN_CAP) break;
        __builtin_amdgcn_s_sleep(POLL_SLEEP);
      }
    } else {
      if (w == 0) {
        for (;;) {
          MEMBAR();
          u32x2 f = LD64(rws, WS_F1 + (b % NREP) * REP_STRIDE + lane * 8, AUX_SC1);
          if (__all(f[0] == tag && f[1] == tag) || ++spins > SPIN_CAP) break;
          __builtin_amdgcn_s_sleep(POLL_SLEEP);
        }
      }
      __builtin_amdgcn_s_barrier();
    }
    MEMBAR();
  }

  PRIO(3);
  {
    u32x2 d[4];
#pragma unroll
    for (int j = 0; j < 4; ++j) d[j] = LD64S(rws, poff, (j ^ pj) * 128, AUX_SC1);
    u32x4 o[2];
#pragma unroll
    for (int c = 0; c < 2; ++c) {
      u32x4 a = {d[0][c], d[1][c], d[2][c], d[3][c]};
      f32x4 dr = mfma16(a, F.re, Z4), di = mfma16(a, F.im, Z4);
      u32x4 bb = pack4(dr * twc[c] + di * tws[c], di * twc[c] - dr * tws[c]);
      o[c] = pack4(mfma16(c ? M1.re : F.re, bb, Z4), mfma16(c ? M1.im : F.im, bb, Z4));
    }
#pragma unroll
    for (int r = 0; r < 4; ++r) *(u32x2*)(lds + L_R1 + wa + 16 * r * XS) = u32x2{o[0][r], o[1][r]};
  }
  CFrag Fc = conjfrag(F), Gc = ldfrag(cl + C_GC * 2048), M1c = ldfrag(cl + C_M1C * 2048);
  twc[2] = *(const f32x4*)(cl + C_TW1C * 2048); tws[2] = *(const f32x4*)(cl + C_TW1C * 2048 + 1024);
  PINF(Fc);

  LDSBAR();
  if (b == 1 && threadIdx.x == 0) ST32(ep + 1, rws, WS_EP, AUX_SC1);
  {
    f32x4 ar[2], ai[2];
#pragma unroll
    for (int tt = 0; tt < 2; ++tt) {
      u32x2 p0 = *(const u32x2*)(lds + L_R1 + tt * (256 * XS) + rp), p1 = *(const u32x2*)(lds + L_R1 + tt * (256 * XS) + rp + 8);
      u32x4 dd = {p0[0], p0[1], p1[0], p1[1]};
      ar[tt] = mfma16(G.re, dd, Z4); ai[tt] = mfma16(G.im, dd, Z4);
    }
    u32x4 y[2];
    if (!special) {
      y[0] = pack4(cf.ar * ar[0] - cf.ai * ai[0] + cf.br * ar[1] - cf.bi * ai[1], cf.ar * ai[0] + cf.ai * ar[0] + cf.br * ai[1] + cf.bi * ar[1]);
      y[1] = pack4(cf.gr * ar[1] - cf.gi * ai[1] - cf.br * ar[0] + cf.bi * ai[0], cf.gr * ai[1] + cf.gi * ar[1] - cf.br * ai[0] - cf.bi * ar[0]);
    } else {
      char* ml = lds + L_W;
#pragma unroll
      for (int tt = 0; tt < 2; ++tt) *(u32x4*)(ml + tt * 16384 + kp * 64 + ((q ^ (kp >> 1)) & 3) * 16) = pack4(ar[tt], ai[tt]);
      const f32x4 b2r = *(const f32x4*)(lds + L_R0 + threadIdx.x * 16), b2i = *(const f32x4*)(lds + L_R0 + 16384 + threadIdx.x * 16);
      LDSBAR();
      const int km = (512 - kp - 256 * (q >> 1)) & 511, kk = km & 255, eh = (km >> 8) * 2;
      f32x4 br[2], bi[2];
#pragma unroll
      for (int tt = 0; tt < 2; ++tt) {
        const char* mr = ml + tt * 16384 + kk * 64;
        const u32x4 m0 = *(const u32x4*)(mr + (((eh + 1 - (q & 1)) ^ (kk >> 1)) & 3) * 16);
        unsigned mm[4];
        if (tt == 1) { mm[0] = m0[3]; mm[1] = m0[2]; mm[2] = m0[1]; mm[3] = m0[0]; }
        else {
          mm[0] = *(const unsigned*)(mr + (((eh + (q & 1)) ^ (kk >> 1)) & 3) * 16);
          mm[1] = m0[3]; mm[2] = m0[2]; mm[3] = m0[1];
        }
#pragma unroll
        for (int r = 0; r < 4; ++r) { br[tt][r] = bflo(mm[r]); bi[tt][r] = bfhi(mm[r]); }
      }
      y[0] = pack4(cf.ar * ar[0] - cf.ai * ai[0] + cf.br * br[0] + cf.bi * bi[0], cf.ar * ai[0] + cf.ai * ar[0] - cf.br * bi[0] + cf.bi * br[0]);
      y[1] = pack4(cf.gr * ar[1] - cf.gi * ai[1] + b2r * br[1] + b2i * bi[1], cf.gr * ai[1] + cf.gi * ar[1] - b2r * bi[1] + b2i * br[1]);
    }
#pragma unroll
    for (int tt = 0; tt < 2; ++tt) {
      u32x4 s = pack4(mfma16(Gc.re, y[tt], Z4), mfma16(Gc.im, y[tt], Z4));
#pragma unroll
      for (int h = 0; h < 2; ++h) *(u32x2*)(lds + L_R0 + tt * (8 * ES) + pp + h * ES) = u32x2{s[2 * h], s[2 * h + 1]};
    }
  }
  LDSBAR();
  {
    u32x2 vv[4];
#pragma unroll
    for (int j = 0; j < 4; ++j) vv[j] = *(const u32x2*)(lds + L_R0 + rd + 128 * j);
    u32x4 o[2];
#pragma unroll
    for (int c = 0; c < 2; ++c) {
      u32x4 a = {vv[0][c], vv[1][c], vv[2][c], vv[3][c]};
      f32x4 dr = mfma16(a, c ? M1c.re : Fc.re, Z4), di = mfma16(a, c ? M1c.im : Fc.im, Z4);
      u32x4 bb = pack4(dr * twc[2 * c] - di * tws[2 * c], di * twc[2 * c] + dr * tws[2 * c]);
      o[c] = pack4(mfma16(Fc.re, bb, Z4), mfma16(Fc.im, bb, Z4));
    }
#pragma unroll
    for (int r = 0; r < 4; ++r) ST64S((u32x2{o[0][r] | 1u, o[1][r]}), rws, poff, (r ^ pj) * 128, AUX_SC1);
  }

  PRIO(0);
  {
    CFrag A3;
#pragma unroll
    for (int j = 0; j < 4; ++j) {
      const int k1 = w + 16 * (4 * q + j);
      setfrag<-1>(A3, j, (float)((8 * idx * k1 + (k1 > 128 ? k1 - 256 : k1) * n2) & 2047) * (1.f / 2048));
      const unsigned sg = k1 > 128 ? 0x80000000u : 0u;
      A3.re[j] ^= sg; A3.im[j] ^= sg;
    }
    unsigned ooff = ((64 * q + w) * 8 + n2) * 4096 + 2048 + cg * 128 + idx * 8;
    PINF(A3); PIN(ooff);
    u32x2 d[4];
    int spins = 0;

    for (;;) {
      MEMBAR();
#pragma unroll
      for (int j = 0; j < 4; ++j) d[j] = LD64S(rws, toff, j * 262144, AUX_SC1);
      const unsigned all = d[0][0] & d[1][0] & d[2][0] & d[3][0];
      if (__all(all & 1u) || ++spins > SPIN_CAP) break;
      __builtin_amdgcn_s_sleep(2);
    }

    PRIO(3);
    u32x4 o[2];
#pragma unroll
    for (int tt = 0; tt < 2; ++tt) {
      u32x4 dd = {d[0][tt], d[1][tt], d[2][tt], d[3][tt]};
      o[tt] = pack4(mfma16(A3.re, dd, Z4), mfma16(A3.im, dd, Z4));
    }
#pragma unroll
    for (int r = 0; r < 4; ++r) *(u32x2*)(lds + L_R1 + ewr + r * ES) = u32x2{o[0][r], o[1][r]};
    LDSBAR();
    u32x2 e[4];
#pragma unroll
    for (int j = 0; j < 4; ++j) e[j] = *(const u32x2*)(lds + L_R1 + erd + j * 128);
#pragma unroll
    for (int tt = 0; tt < 2; ++tt) {
      u32x4 vv = {e[0][tt], e[1][tt], e[2][tt], e[3][tt]};
      o[tt] = pack4(mfma16(Fc.re, vv, Z4), mfma16(Fc.im, vv, Z4));
    }
    const rsrc_t ro = mkrsrc(P.out, 2048 * 2048 * 2);
#pragma unroll
    for (int r = 0; r < 4; ++r) ST64S((u32x2{o[0][r], o[1][r]}), ro, ooff, r * 524288, OUT_AUX);
  }

}

template <int V> struct IC { static constexpr int value = V; };
template <int I, int N, class Fn>
DI void static_for(Fn&& fn) { if constexpr (I < N) { fn(IC<I>{}); static_for<I + 1, N>(fn); } }
struct CZ { f32x2 z, s; };
DI CZ mkcz(f32x2 z) { return CZ{z, f32x2{-z.y, z.x}}; }
DI CZ mkczc(f32x2 z) { return CZ{f32x2{z.x, -z.y}, f32x2{z.y, z.x}}; }
DI f32x2 cmul(f32x2 a, const CZ& c) { return __builtin_elementwise_fma(a.yy, c.s, a.xx * c.z); }
DI f32x2 csq(f32x2 a) { return f32x2{__builtin_fmaf(a.x, a.x, -(a.y * a.y)), (a.x + a.x) * a.y}; }
DI f32x2 cpow2(float lr2, float rh, float rl, float e) {
  float m = __builtin_amdgcn_exp2f(lr2 * e);
  float fr = __builtin_fmaf(rl, e, __builtin_amdgcn_fractf(rh * e));
  return f32x2{cosr(fr), sinr(fr)} * m;
}
DI float exp_acc(float x) {
  const float t = x * 1.44269504088896341f;
  const float e = __builtin_fmaf(x, 1.44269504088896341f, -t) + x * 1.925963033500011e-08f;
  const float r = __builtin_amdgcn_exp2f(t);
  return __builtin_fmaf(r, e * 0.693147180559945f, r);
}
template <int O, int S>
DI void strow4(unsigned base, unsigned a, unsigned b, unsigned c, unsigned d) {
  asm volatile("s_mov_b32 m0, %4\n\ts_nop 0\n\tds_write_addtid_b32 %0 offset:%5\n\tds_write_addtid_b32 %1 offset:%6\n\t"
               "ds_write_addtid_b32 %2 offset:%7\n\tds_write_addtid_b32 %3 offset:%8"
               :: "v"(a), "v"(b), "v"(c), "v"(d), "s"(base), "i"(O), "i"(O + S), "i"(O + 2 * S), "i"(O + 3 * S) : "memory");
}
template <int O>
DI void strow1(unsigned base, unsigned a) {
  asm volatile("s_mov_b32 m0, %1\n\ts_nop 0\n\tds_write_addtid_b32 %0 offset:%2" :: "v"(a), "s"(base), "i"(O) : "memory");
}
#define U_ROT 2
#define U_EARLY 1
#define U_AUX 0
#define PD 4

DI void role_ssm(const Params& P, char* lds, const int a) {
  char* ws = P.ws; (void)ws;
  const int d0 = 8 * ((a & 7) * 16 + (a >> 3));
  const int tid = threadIdx.x, lane = tid & 63, w = __builtin_amdgcn_readfirstlane(tid >> 6), idx = lane & 15, q = lane >> 4;
  const int ch = w & 7, hf = w >> 3;

  const rsrc_t ru = mkrsrc(P.u, 2048 * 1024 * 2);
  const rsrc_t ro = mkrsrc(P.out, 2048 * 2048 * 2);
  const int pn = (d0 + ch) * 128 + hf * 64 + lane;
  const unsigned pa = P.logA[pn], pi = P.Aim[pn], pr = P.Cr[pn], pc = P.Ci[pn], pd = P.logdt[d0 + ch], pk = P.Dskip[d0 + ch];
  __builtin_amdgcn_s_barrier();
  u32x4 uv[2];
  const int wr = (w + U_ROT * (a >> 3)) & 15;
  const unsigned uoff = (128 * wr + 2 * lane) * 2048 + d0 * 2;
  if (U_EARLY == 2 || hf == 0) {
#pragma unroll
    for (int r = 0; r < 2; ++r) uv[r] = LD128(ru, uoff + r * 2048, U_AUX);
  }
  *(u32x4*)(lds + A_K + tid * 16) = u32x4{0, 0, 0, 0};
  if (hf) {
    char* z = lds + A_UL + ch * A_ULS;
    *(u32x2*)(z + lane * 8) = u32x2{0, 0};
    if (lane < 4) *(u32x2*)(z + (lane == 0 ? 512 : 4608 + lane * 8)) = u32x2{0, 0};
  } else if (w < 5) {
    const int lo = (0x09072 >> (4 * w)) & 15, hi = (0x6F896 >> (4 * w)) & 15;
    const unsigned bits = (((2u << (hi - lo)) - 1u) << (idx + lo)) >> (8 * q);
    const unsigned val = w == 4 ? 0xffffffffu : 0x3f803f80u;
    u32x4 f;
#pragma unroll
    for (int j = 0; j < 4; ++j)
      f[j] = plo((unsigned)__builtin_amdgcn_sbfe((int)bits, 2 * j, 1), (unsigned)__builtin_amdgcn_sbfe((int)bits, 2 * j + 1, 1)) & val;
    *(u32x4*)(lds + A_M + w * 1024 + lane * 16) = f;
  }
  char* st = lds + w * A_STS;
  const unsigned stb = __builtin_amdgcn_readfirstlane((unsigned)(size_t)st);
  f32x4 D[2][2];
  {
    const float dt = rbf(exp_acc(bflo(pd)));
    const float lr2 = rbf(exp_acc(bflo(pa))) * dt * -1.44269504088896341f;
    const float th = bflo(pi) * dt;

    const float ihi = 0.15915494309189535f, ilo = 6.4206382e-09f;
    const float rh = th * ihi, rl = __builtin_fmaf(th, ilo, __builtin_fmaf(th, ihi, -rh));
    const f32x2 z1 = cpow2(lr2, rh, rl, 1.f), z2 = csq(z1), z8 = csq(csq(z2));
    const CZ c32 = mkcz(cpow2(lr2, rh, rl, 32.f));
    const char* sr = st + idx * 272 + q * 16;
    u32x4 Bf[2][4];
    f32x2 c4[4];
    c4[1] = z8; c4[2] = csq(z8); c4[3] = cmul(c4[2], mkcz(z8));
    strow4<0, 272>(stb, 0x3f80u, pack2v(c4[1]), pack2v(c4[2]), pack2v(c4[3]));
    c4[0] = c32.z; c4[1] = cmul(c4[1], c32); c4[2] = cmul(c4[2], c32); c4[3] = cmul(c4[3], c32);
#define BSTEP(S, MORE) do { strow4<4 * S * 272, 272>(stb, pack2v(c4[0]), pack2v(c4[1]), pack2v(c4[2]), pack2v(c4[3])); \
      if (MORE) { c4[0] = cmul(c4[0], c32); c4[1] = cmul(c4[1], c32); c4[2] = cmul(c4[2], c32); c4[3] = cmul(c4[3], c32); } } while (0)
    BSTEP(1, 1); BSTEP(2, 1); BSTEP(3, 1);
#pragma unroll
    for (int ks = 0; ks < 4; ++ks) Bf[0][ks] = *(const u32x4*)(sr + ks * 64);
    const f32x2 z256 = csq(c4[0]);
    BSTEP(0, 1); BSTEP(1, 1); BSTEP(2, 1); BSTEP(3, 0);
#pragma unroll
    for (int ks = 0; ks < 4; ++ks) Bf[1][ks] = *(const u32x4*)(sr + ks * 64);

    if (U_EARLY == 1 && hf == 1) {
#pragma unroll
      for (int r = 0; r < 2; ++r) uv[r] = LD128(ru, uoff + r * 2048, U_AUX);
    }
    const CZ c1 = mkczc(z1), c2 = mkczc(z2), c256 = mkczc(z256), c512 = mkczc(csq(z256));
    const f32x2 C = f32x2{bflo(pr), -bflo(pc)} * (dt + dt);
    f32x2 a0 = C, a1 = cmul(C, c1), s0 = cmul(C, c256);
    f32x2 a2 = cmul(a0, c2), a3 = cmul(a1, c2), s1 = cmul(s0, c2);
    strow4<0, 272>(stb, pack2v(a0), pack2v(a1), pack2v(a2), pack2v(a3));
    a0 = cmul(a2, c2); a1 = cmul(a3, c2); f32x2 s2 = cmul(s1, c2);
    a2 = cmul(a0, c2); a3 = cmul(a1, c2); f32x2 s3 = cmul(s2, c2);
    strow4<4 * 272, 272>(stb, pack2v(a0), pack2v(a1), pack2v(a2), pack2v(a3));
    f32x2 s4 = cmul(s3, c2), t516 = cmul(s2, c256), t772 = cmul(s2, c512);
    strow4<8 * 272, 272>(stb, pack2v(s0), pack2v(s1), pack2v(s2), pack2v(s3));
    strow1<12 * 272>(stb, pack2v(s4));
    const int rw0 = idx < 8 ? 7 - idx : 8 + ((0x43222100u >> (4 * ((15 - idx) & 7))) & 7);
    const char* p0 = st + rw0 * 272 + q * 16;
    u32x4 f0[4];
#pragma unroll
    for (int ks = 0; ks < 4; ++ks) f0[ks] = *(const u32x4*)(p0 + ks * 64);
    f32x2 t520 = cmul(s4, c256), t776 = cmul(s4, c512);
    strow4<0, 272>(stb, pack2v(t520), pack2v(t516), pack2v(t776), pack2v(t772));
    f32x2 e0 = cmul(t520, c512), e1 = cmul(t776, c512);
    f32x2 e2 = cmul(e0, c512), e3 = cmul(e1, c512);
    strow4<4 * 272, 272>(stb, pack2v(e0), pack2v(e1), pack2v(e2), pack2v(e3));
    const char* p1 = st + (idx & 7) * 272 + q * 16;
#pragma unroll
    for (int tb = 0; tb < 2; ++tb) { D[0][tb] = Z4; D[1][tb] = Z4; }
#pragma unroll
    for (int ks = 0; ks < 4; ++ks) {
      u32x4 f1 = *(const u32x4*)(p1 + ks * 64);
#pragma unroll
      for (int tb = 0; tb < 2; ++tb) { D[0][tb] = mfma16(f0[ks], Bf[tb][ks], D[0][tb]); D[1][tb] = mfma16(f1, Bf[tb][ks], D[1][tb]); }
    }
  }
  if (U_EARLY == 0 && hf == 1) {
#pragma unroll
    for (int r = 0; r < 2; ++r) uv[r] = LD128(ru, uoff + r * 2048, U_AUX);
  }
  *(f32x4*)(st + lane * 16) = hf ? D[0][0] : D[1][0];
  *(f32x4*)(st + 1024 + lane * 16) = hf ? D[0][1] : D[1][1];

  LDSBAR();

  char* kk = lds + A_K + ch * 2048;
  const char* ul = lds + A_UL + ch * A_ULS;
  const char* ub = ul + 512 + 32 * idx + 16 * q;
  u32x4 R[17];
  {
    const char* ps = lds + (w ^ 8) * A_STS + lane * 16;
    unsigned ka[2], kc[2];
#pragma unroll
    for (int tb = 0; tb < 2; ++tb) {
      f32x4 k = (hf ? D[1][tb] : D[0][tb]) + *(const f32x4*)(ps + tb * 1024);
      if (tb == 0) k[3] += (hf == 0 && lane == 16) ? bflo(pk) : 0.f;
      ka[tb] = pack2(k[0], k[1]); kc[tb] = pack2(k[2], k[3]);
    }
    if (hf == 0) {
      const int x = 531 - 7 - 256 * (q >> 1) - 8 * idx + 4 * (q & 1);
      *(u32x2*)(kk + 2 * x) = u32x2{ka[0], kc[0]};
      *(u32x2*)(kk + 2 * x - 256) = u32x2{ka[1], kc[1]};
      if (lane == 47) *(u32x2*)(kk + 2 * 16) = u32x2{ka[1] << 16, plo(ka[1], ka[1])};
      const int x0 = 527 - idx + 8 * q;
      const unsigned sh = (x0 & 1) * 16;
      const unsigned* hp = (const unsigned*)(kk + 4 * (x0 >> 1) - 1024);
#pragma unroll
      for (int g = 0; g < 17; g += 9) {
        unsigned ex[9];
#pragma unroll
        for (int m = g; m < g + 9 && m < 17; ++m) {
#pragma unroll
          for (int j = 0; j < 4; ++j) R[m][j] = hp[j + 16 * (16 - m)];
          ex[m - g] = hp[4 + 16 * (16 - m)];
        }
#pragma unroll
        for (int m = g; m < g + 9 && m < 17; ++m) {
#pragma unroll
          for (int j = 0; j < 4; ++j) R[m][j] = __builtin_amdgcn_alignbit(j < 3 ? R[m][j + 1] : ex[m - g], R[m][j], sh);
        }
        SCHED();
      }
    } else {
      if (q == 0) {
        if (idx == 15) { *(unsigned*)(kk + A_K1024) = plo(kc[1], kc[1]); kc[1] &= 0xffff0000u; }
        unsigned* r2 = (unsigned*)(kk + A_R2) - idx;
        r2[95] = ka[0]; r2[63] = kc[0]; r2[79] = ka[1]; r2[47] = kc[1];
      }
      if (q == 1) {
        uint16_t* r3 = (uint16_t*)(kk + A_R3) - idx;
#pragma unroll
        for (int tb = 0; tb < 2; ++tb) {
          r3[127 - 16 * tb] = (uint16_t)ka[tb]; r3[95 - 16 * tb] = (uint16_t)(ka[tb] >> 16);
          r3[63 - 16 * tb] = (uint16_t)kc[tb]; r3[31 - 16 * tb] = (uint16_t)(kc[tb] >> 16);
        }
      }
#pragma unroll
      for (int dl = 3; dl <= 8; ++dl) {
        const char* p = kk + A_R2 + 2 * (316 - 32 * dl - 4 * idx + 8 * q);
        u32x2 lo = *(const u32x2*)p, hi = *(const u32x2*)(p + 8);
        R[dl - 3] = u32x4{lo[0], lo[1], hi[0], hi[1]};
      }
#pragma unroll
      for (int dl = 4; dl <= 7; ++dl) {
        const unsigned* p = (const unsigned*)(kk + A_R3 + 2 * (254 - 32 * dl - 2 * idx + 8 * q));
        R[2 + dl] = u32x4{p[0], p[1], p[2], p[3]};
      }
      const char* ml = lds + A_M + lane * 16;
#pragma unroll
      for (int i = 0; i < 5; ++i) R[10 + i] = *(const u32x4*)(ml + 1024 * i);
      const unsigned k1024 = *(const unsigned*)(kk + A_K1024);
      R[14] &= u32x4{k1024, k1024, k1024, k1024};
      R[15] = R[14]; R[16] = R[14];
    }
  }
#pragma unroll
  for (int m = 0; m < 17; ++m) PIN(R[m]);

  {
    const unsigned base = __builtin_amdgcn_readfirstlane((unsigned)(size_t)lds + 40000u + 256u * wr);
    constexpr int O = A_UL + 520 - 40000;
    strow4<O, A_ULS>(base, plo(uv[0][0], uv[1][0]), phi(uv[0][0], uv[1][0]), plo(uv[0][1], uv[1][1]), phi(uv[0][1], uv[1][1]));
    strow4<O + 4 * A_ULS, A_ULS>(base, plo(uv[0][2], uv[1][2]), phi(uv[0][2], uv[1][2]), plo(uv[0][3], uv[1][3]), phi(uv[0][3], uv[1][3]));
    LDSBAR();
  }

  f32x4 acc[8];
#pragma unroll
  for (int t = 0; t < 8; ++t) acc[t] = Z4;
  if (hf == 0) {
    u32x4 ring[PD + 1];
#pragma unroll
    for (int i = 0; i < PD; ++i) ring[i] = *(const u32x4*)(ub + 64 * (i - 8));
    f32x4 part;
    const char* pt = lds + A_PT + ch * 2048 + lane * 16;
    char* yw = lds + A_Y + ch * A_YS + 32 * idx + 8 * q;
    static_for<-8, 57>([&](auto fc) {
      constexpr int f = decltype(fc)::value;
      if constexpr (f + PD <= 56) ring[(f + 8 + PD) % (PD + 1)] = *(const u32x4*)(ub + 64 * (f + PD));
      if constexpr ((f & 7) == 6 && f >= 14) {
        constexpr int al = (f + 2) / 8;
        part = *(const f32x4*)(pt + ((al >> 1) & 1) * 16384 + (al & 1) * 1024);
      }
      static_for<0, 8>([&](auto ac) {
        constexpr int al = decltype(ac)::value, m = 8 * al - f;
        if constexpr (m >= 0 && m <= 16) acc[al] = mfma16(R[m], ring[(f + 8) % (PD + 1)], acc[al]);
      });
      if constexpr ((f > 0 && (f & 7) == 1) || f == 56) {
        constexpr int al = f / 8;
        f32x4 y = acc[al];
        if constexpr (al >= 2) y += part;
        *(u32x2*)(yw + ((al >> 1) & 1) * (8 * A_YS) + (al & 1) * 512) = u32x2{pack2(y[0], y[1]), pack2(y[2], y[3])};
        if constexpr (al & 1) LDSBAR();
      }
      SCHED();
    });
  } else {
    const char* wc = ul + 480 + 32 * idx + 16 * q;
    const char* wb = ul + 480 + 16 * idx + 16 * q;
    const char* yr = lds + A_Y + (idx >> 2) * A_YS + (64 * ch + 16 * q + 4 * (idx & 3)) * 2;
    const unsigned eo = (64 * ch + lane) * 4096 + d0 * 2;
#pragma unroll
    for (int c = 0; c < 5; ++c) {
      if (c > 0) {
        u32x2 e0 = trread(yr + ((c - 1) & 1) * (8 * A_YS)), e1 = trread(yr + ((c - 1) & 1) * (8 * A_YS) + 4 * A_YS);
        __builtin_amdgcn_raw_buffer_store_b128(u32x4{e0[0], e0[1], e1[0], e1[1]}, ro, eo, (c - 1) * (512 * 4096), 0);
      }
      if (c < 3) {
        u32x4 wn[4], w3[2], uh[2];
#pragma unroll
        for (int i = 0; i < 4; ++i) wn[i] = *(const u32x4*)(wb + 256 * (4 * c + i));
#pragma unroll
        for (int i = 0; i < 2; ++i) {
          if (c > 0) w3[i] = *(const u32x4*)(wc + 512 * (2 * c - 2 + i));
          if (2 * c + 2 + i >= 3) uh[i] = *(const u32x4*)(ub + 64 * (8 * (2 * c + 2 + i) - 32));
        }
        u32x4 v[4], v3[2];
#pragma unroll
        for (int i = 0; i < 4; ++i) v[i] = pack4(mfma16(wn[i], R[10], Z4), mfma16(wn[i], R[11], Z4));
        if (c > 0) {
#pragma unroll
          for (int i = 0; i < 2; ++i) v3[i] = pack4(mfma16(w3[i], R[12], Z4), mfma16(w3[i], R[13], Z4));
        }
#pragma unroll
        for (int i = 0; i < 2; ++i) if (2 * c + 2 + i >= 3) acc[2 * c + 2 + i] = mfma16(R[14], uh[i], acc[2 * c + 2 + i]);
#pragma unroll
        for (int i = 0; i < 4; ++i)
#pragma unroll
          for (int al = 2; al < 8; ++al) {
            const int dl = 2 * al - (4 * c + i);
            if (dl >= 3 && dl <= 8) acc[al] = mfma16(v[i], R[dl - 3], acc[al]);
          }
        if (c > 0) {
#pragma unroll
          for (int i = 0; i < 2; ++i)
#pragma unroll
            for (int al = 2 * c + 2 + i; al < 8; ++al) acc[al] = mfma16(v3[i], R[6 + al - (2 * c - 2 + i) - 4], acc[al]);
        }
        char* pt = lds + A_PT + ((c + 1) & 1) * 16384 + ch * 2048 + lane * 16;
        *(f32x4*)pt = acc[2 * c + 2];
        *(f32x4*)(pt + 1024) = acc[2 * c + 3];
      }
      if (c < 4) LDSBAR();
    }
  }

}

__global__ __launch_bounds__(1024, 4) void fused(Params P) {
  extern __shared__ __attribute__((aligned(16))) char lds[];
  const int b = blockIdx.x;
  if (b < 128) { if (ROLE_MASK & 1) role_spectral(P, lds, b); }
  else { if (ROLE_MASK & 2) role_ssm(P, lds, b - 128); }
}

extern "C" void kernel_launch(void* const* d_in, const int* in_sizes, int n_in,
                              void* d_out, int out_size, void* d_ws, size_t ws_size,
                              hipStream_t stream) {
  Params P;
  P.u = (const uint16_t*)d_in[0]; P.logA = (const uint16_t*)d_in[1]; P.Aim = (const uint16_t*)d_in[2]; P.Cr = (const uint16_t*)d_in[3];
  P.Ci = (const uint16_t*)d_in[4]; P.logdt = (const uint16_t*)d_in[5]; P.Dskip = (const uint16_t*)d_in[6]; P.x = (const uint16_t*)d_in[7];
  P.wr = (const uint16_t*)d_in[8]; P.wi = (const uint16_t*)d_in[9]; P.out = (uint16_t*)d_out; P.ws = (char*)d_ws;
  (void)hipFuncSetAttribute((const void*)fused, hipFuncAttributeMaxDynamicSharedMemorySize, LDS_BYTES);
  fused<<<256, 1024, LDS_BYTES, stream>>>(P);
}
```

```cpp
#include <hip/hip_runtime.h>
#include <hip/hip_bf16.h>
#include <stdint.h>

typedef __attribute__((ext_vector_type(8))) __bf16 bf16x8;
typedef __attribute__((ext_vector_type(2))) __bf16 bf16x2;
typedef __attribute__((ext_vector_type(2))) float f32x2;
typedef __attribute__((ext_vector_type(4))) float f32x4;
typedef __attribute__((ext_vector_type(4))) unsigned u32x4;
typedef __attribute__((ext_vector_type(2))) unsigned u32x2;
typedef __attribute__((ext_vector_type(4))) short s16x4;
#define LAS __attribute__((address_space(3)))
#define DI __device__ __forceinline__

#define ROLE_MASK 3

#define MEMBAR() asm volatile("" ::: "memory")
#define PIN(x) asm volatile("" : "+v"(x))
#define PINF(f) do { PIN((f).re); PIN((f).im); } while (0)
#define SCHED() __builtin_amdgcn_sched_barrier(0)
#define DRAIN() asm volatile("s_waitcnt vmcnt(0)" ::: "memory")
#define AUX_NT 0
#define AUX_SC1 16
typedef __amdgpu_buffer_rsrc_t rsrc_t;
DI rsrc_t mkrsrc(const void* p, unsigned bytes) { return __builtin_amdgcn_make_buffer_rsrc((void*)p, 0, bytes, 0x00020000); }
#define LD64S(r, off, soff, aux) __builtin_amdgcn_raw_buffer_load_b64(r, off, soff, aux)
#define ST64S(v, r, off, soff, aux) __builtin_amdgcn_raw_buffer_store_b64(v, r, off, soff, aux)
#define LDSBAR() do { asm volatile("s_waitcnt lgkmcnt(0)" ::: "memory"); __builtin_amdgcn_s_barrier(); MEMBAR(); } while (0)
#define LD32(r, off, aux) __builtin_amdgcn_raw_buffer_load_b32(r, off, 0, aux)
#define LD64(r, off, aux) __builtin_amdgcn_raw_buffer_load_b64(r, off, 0, aux)
#define LD128(r, off, aux) __builtin_amdgcn_raw_buffer_load_b128(r, off, 0, aux)
#define ST32(v, r, off, aux) __builtin_amdgcn_raw_buffer_store_b32(v, r, off, 0, aux)
#define ST64(v, r, off, aux) __builtin_amdgcn_raw_buffer_store_b64(v, r, off, 0, aux)
#define ST128(v, r, off, aux) __builtin_amdgcn_raw_buffer_store_b128(v, r, off, 0, aux)

DI unsigned pack2(float a, float b) { f32x2 v = {a, b}; bf16x2 r = __builtin_convertvector(v, bf16x2); return __builtin_bit_cast(unsigned, r); }
DI unsigned pack2v(f32x2 v) { bf16x2 r = __builtin_convertvector(v, bf16x2); return __builtin_bit_cast(unsigned, r); }
DI f32x4 mfma16(u32x4 a, u32x4 b, f32x4 c) {
  return __builtin_amdgcn_mfma_f32_16x16x32_bf16(__builtin_bit_cast(bf16x8, a), __builtin_bit_cast(bf16x8, b), c, 0, 0, 0);
}
DI float bflo(unsigned v) { return __uint_as_float(v << 16); }
DI float bfhi(unsigned v) { return __uint_as_float(v & 0xffff0000u); }
DI float rbf(float x) { return bflo(pack2(x, 0.f)); }
DI float cosr(float rev) { return __builtin_amdgcn_cosf(rev); }
DI float sinr(float rev) { return __builtin_amdgcn_sinf(rev); }
DI float mirror16(float v) { return __builtin_bit_cast(float, __builtin_amdgcn_update_dpp(0, __builtin_bit_cast(int, v), 0x140, 0xf, 0xf, true)); }
DI f32x4 mirror4(f32x4 v) { return f32x4{mirror16(v[0]), mirror16(v[1]), mirror16(v[2]), mirror16(v[3])}; }
DI u32x4 pack4(f32x4 re, f32x4 im) { return u32x4{pack2(re[0], im[0]), pack2(re[1], im[1]), pack2(re[2], im[2]), pack2(re[3], im[3])}; }
DI unsigned plo(unsigned a, unsigned b) { return __builtin_amdgcn_perm(b, a, 0x05040100u); }
DI unsigned phi(unsigned a, unsigned b) { return __builtin_amdgcn_perm(b, a, 0x07060302u); }
static constexpr f32x4 Z4 = {0.f, 0.f, 0.f, 0.f};

struct CFrag { u32x4 re, im; };
template <int SGN>
DI void setfrag(CFrag& f, int j, float rev) {
  float c = cosr(rev), s = sinr(rev);
  if (SGN > 0) { f.re[j] = pack2(c, s); f.im[j] = pack2(-s, c); }
  else { f.re[j] = pack2(c, -s); f.im[j] = pack2(s, c); }
}
DI CFrag conjfrag(const CFrag& f) {
  CFrag g;
#pragma unroll
  for (int j = 0; j < 4; ++j) { g.re[j] = f.re[j] ^ 0x80000000u; g.im[j] = f.im[j] ^ 0x00008000u; }
  return g;
}

#define WS_T 0u
#define WS_F1 (4u << 20)
#define WS_EP (WS_F1 + 786432u)
#define WS_BYTES (5u << 20)
#define SETUP_FIRST 0
#define USE_PRIO 1
#define PRIO(n) do { if (USE_PRIO) __builtin_amdgcn_s_setprio(n); } while (0)
#define POLL2_SLEEP 2
#define POLL2_PRE 0
#define W_MODE 0
#define PRE_SLEEP 8
#define W_LATE 0
#define W_POS 2
#define T_SWZ 1
#define POLL_MODE 0
#define NREP 32
#define REP_STRIDE 512
#define POLL_SLEEP 1
#define SPIN_CAP (1 << 18)
#define OUT_AUX 16
DI unsigned mktag(unsigned e) { return (e ^ 0x5bd1e995u) * 0x9E3779B1u + 0x7F4A7C15u; }

#define XS 72
#define ES 2560
#define L_R0 0
#define L_R1 40960
#define L_W 81920
#define WRS 1056
#define W_EX (32 * WRS)
#define L_C (L_W + 33920)
#define L_SV (L_C + 16384)
#define A_STS 4352
#define A_UL (16 * A_STS)
#define A_ULS 4640
#define A_K (A_UL + 8 * A_ULS)
#define A_CS 1216
#define A_R2 (4 * A_CS)
#define A_R3 (A_R2 + 512)
#define A_KS 5696
#define A_M (A_K + 8 * A_KS)
#define A_PT 2176
#define LDS_BYTES (A_M + 6144)

struct Params {
  const uint16_t *u, *logA, *Aim, *Cr, *Ci, *logdt, *Dskip, *x, *wr, *wi;
  uint16_t* out;
  char* ws;
};
DI u32x2 ld8(const char* p) { return *(const u32x2*)p; }
DI void st8(char* p, u32x2 v) { *(u32x2*)p = v; }
DI u32x4 ld16(const char* p0, const char* p1) { const u32x2 lo = ld8(p0), hi = ld8(p1); return u32x4{lo[0], lo[1], hi[0], hi[1]}; }
DI u32x4 xor4(u32x4 a, unsigned m) { return a ^ u32x4{m, m, m, m}; }
DI unsigned mirru(unsigned v) { return (unsigned)__builtin_amdgcn_update_dpp(0, (int)v, 0x140, 0xf, 0xf, true); }
DI f32x4 unpk4(u32x2 v) { return f32x4{bflo(v[0]), bfhi(v[0]), bflo(v[1]), bfhi(v[1])}; }
DI u32x2 trread(const char* p) { return __builtin_bit_cast(u32x2, __builtin_amdgcn_ds_read_tr16_b64_v4i16((LAS s16x4*)p)); }
DI CFrag ldfrag(const char* p) { CFrag f; f.re = *(const u32x4*)p; f.im = *(const u32x4*)(p + 1024); return f; }

enum { C_GQ = 0, C_M1, C_TW0, C_TW1, C_G, C_M1C, C_TW1C, C_GC };

DI void dma16(rsrc_t r, unsigned voff, unsigned soff, unsigned ldsaddr) {
  asm volatile("s_mov_b32 m0, %3\n\ts_nop 0\n\tbuffer_load_dwordx4 %0, %1, %2 offen lds" :: "v"(voff), "s"(r), "s"(soff), "s"(ldsaddr) : "memory");
}
struct WSet { f32x4 r[2], i[2]; };
DI float ldbf(const char* p) { return bflo(*(const uint16_t*)p); }
static_assert(WRS == 1056, "offsets below");
template <bool MIRROR>
DI WSet read_filter(unsigned p1, unsigned p2, unsigned p3) {
  float t[16];
  if (MIRROR)
    asm volatile("ds_read_u16_d16_hi %0, %16 offset:0\n\tds_read_u16_d16_hi %1, %16 offset:1056\n\tds_read_u16_d16_hi %2, %16 offset:2112\n\tds_read_u16_d16_hi %3, %16 offset:3168\n\tds_read_u16_d16_hi %4, %17 offset:0\n\tds_read_u16_d16_hi %5, %18 offset:2112\n\tds_read_u16_d16_hi %6, %18 offset:1056\n\tds_read_u16_d16_hi %7, %18 offset:0\n\tds_read_u16_d16_hi %8, %16 offset:16896\n\tds_read_u16_d16_hi %9, %16 offset:17952\n\tds_read_u16_d16_hi %10, %16 offset:19008\n\tds_read_u16_d16_hi %11, %16 offset:20064\n\tds_read_u16_d16_hi %12, %17 offset:16896\n\tds_read_u16_d16_hi %13, %18 offset:19008\n\tds_read_u16_d16_hi %14, %18 offset:17952\n\tds_read_u16_d16_hi %15, %18 offset:16896\n\ts_waitcnt lgkmcnt(0)"
                 : "=&v"(t[0]), "=&v"(t[1]), "=&v"(t[2]), "=&v"(t[3]), "=&v"(t[4]), "=&v"(t[5]), "=&v"(t[6]), "=&v"(t[7]), "=&v"(t[8]), "=&v"(t[9]), "=&v"(t[10]), "=&v"(t[11]), "=&v"(t[12]), "=&v"(t[13]), "=&v"(t[14]), "=&v"(t[15]) : "v"(p1), "v"(p2), "v"(p3) : "memory");
  else
    asm volatile("ds_read_u16_d16_hi %0, %16 offset:0\n\tds_read_u16_d16_hi %1, %16 offset:1056\n\tds_read_u16_d16_hi %2, %16 offset:2112\n\tds_read_u16_d16_hi %3, %16 offset:3168\n\tds_read_u16_d16_hi %4, %17 offset:0\n\tds_read_u16_d16_hi %5, %17 offset:1056\n\tds_read_u16_d16_hi %6, %17 offset:2112\n\tds_read_u16_d16_hi %7, %17 offset:3168\n\tds_read_u16_d16_hi %8, %16 offset:16896\n\tds_read_u16_d16_hi %9, %16 offset:17952\n\tds_read_u16_d16_hi %10, %16 offset:19008\n\tds_read_u16_d16_hi %11, %16 offset:20064\n\tds_read_u16_d16_hi %12, %17 offset:16896\n\tds_read_u16_d16_hi %13, %17 offset:17952\n\tds_read_u16_d16_hi %14, %17 offset:19008\n\tds_read_u16_d16_hi %15, %17 offset:20064\n\ts_waitcnt lgkmcnt(0)"
                 : "=&v"(t[0]), "=&v"(t[1]), "=&v"(t[2]), "=&v"(t[3]), "=&v"(t[4]), "=&v"(t[5]), "=&v"(t[6]), "=&v"(t[7]), "=&v"(t[8]), "=&v"(t[9]), "=&v"(t[10]), "=&v"(t[11]), "=&v"(t[12]), "=&v"(t[13]), "=&v"(t[14]), "=&v"(t[15]) : "v"(p1), "v"(p2) : "memory");
  WSet s;
  s.r[0] = f32x4{t[0], t[1], t[2], t[3]}; s.r[1] = f32x4{t[4], t[5], t[6], t[7]};
  s.i[0] = f32x4{t[8], t[9], t[10], t[11]}; s.i[1] = f32x4{t[12], t[13], t[14], t[15]};
  return s;
}
struct Coef { f32x4 ar, ai, gr, gi, br, bi; };
DI Coef mkcoef(const WSet& s, float ss, float cc, float sc) {
  Coef c;
  c.ar = s.r[0] * ss + s.r[1] * cc; c.ai = s.i[0] * ss - s.i[1] * cc;
  c.gr = s.r[0] * cc + s.r[1] * ss; c.gi = s.i[0] * cc - s.i[1] * ss;
  c.br = (s.i[1] + s.i[0]) * -sc; c.bi = (s.r[0] - s.r[1]) * sc;
  return c;
}
#define WROW(r) ((r) * WRS + ((r) & 4) * 4)
#define IS 1040

DI void role_spectral(const Params& P, char* lds, const int b) {
  char* ws = P.ws;
  const int lane = threadIdx.x & 63, w = __builtin_amdgcn_readfirstlane(threadIdx.x >> 6), idx = lane & 15, q = lane >> 4;
  const int hb = w >> 3, w8 = w & 7;

  const rsrc_t rws = mkrsrc(ws, WS_BYTES);
  const rsrc_t rx = mkrsrc(P.x, 2048 * 1024 * 2);
  const rsrc_t rwr = mkrsrc(P.wr, 2048 * 513 * 2);
  const rsrc_t rwi = mkrsrc(P.wi, 2048 * 513 * 2);
  const int n2 = b & 7, cg = b >> 3;
  const bool special = b == 0;
  const unsigned lb = __builtin_amdgcn_readfirstlane((unsigned)(size_t)lds);
  auto wrow_of = [&](int r) { return special ? 128 * (r >> 3) + 256 * (r & 7) : ((r >> 3) ? 2048 - b - 256 * (r & 7) : b + 256 * (r & 7)); };
  auto load_filter = [&](int r) {
    const unsigned off = (wrow_of(r) * 1026) & ~15;
    const unsigned dst = lb + L_W + WROW(r);
    dma16(rwr, lane * 16, off, dst);
    dma16(rwi, lane * 16, off, dst + 16 * WRS);
  };
  const unsigned xoff = ((64 * q + w) * 8 + n2) * 2048 + cg * 128 + idx * 8;
  unsigned toff = xoff ^ (T_SWZ * w * 128);
  u32x2 v[4];
#pragma unroll
  for (int j = 0; j < 4; ++j) v[j] = LD64S(rx, xoff, j * 262144, AUX_NT);
  const unsigned ep = LD32(rws, WS_EP, AUX_SC1);
  char* cl = lds + L_C + lane * 16;
  CFrag F;
#pragma unroll
  for (int j = 0; j < 4; ++j) setfrag<1>(F, j, (float)(((4 * q + j) * idx) & 15) * (1.f / 16));
  int ewr = (4 * q) * ES + (w + (w >> 2)) * 128 + idx * 8;
  int erd = w * ES + (5 * q) * 128 + idx * 8;
  CFrag A2;
  {
    const int k1 = w + 16 * idx;
    const int tn = (k1 > 128 ? k1 - 256 : k1) * n2;
#pragma unroll
    for (int j = 0; j < 4; ++j) setfrag<1>(A2, j, (float)((8 * (4 * q + j) * k1 + tn) & 2047) * (1.f / 2048));
    A2.im = xor4(A2.im, k1 > 128 ? 0x80008000u : 0u);
  }
  PINF(A2); PIN(ewr); PIN(erd); PIN(toff);
  {
    PINF(F);
    SCHED();

    PRIO(3);
    u32x4 o[2];
#pragma unroll
    for (int tt = 0; tt < 2; ++tt) {
      u32x4 dd = {v[0][tt], v[1][tt], v[2][tt], v[3][tt]};
      o[tt] = pack4(mfma16(F.re, dd, Z4), mfma16(F.im, dd, Z4));
    }
#pragma unroll
    for (int r = 0; r < 4; ++r) st8(lds + L_R0 + ewr + r * ES, u32x2{o[0][r], o[1][r]});

    LDSBAR();
    u32x2 e[4];
#pragma unroll
    for (int j = 0; j < 4; ++j) e[j] = ld8(lds + L_R0 + erd + j * 128);
#pragma unroll
    for (int tt = 0; tt < 2; ++tt) {
      u32x4 vv = {e[0][tt], e[1][tt], e[2][tt], e[3][tt]};
      o[tt] = pack4(mfma16(A2.re, vv, Z4), mfma16(A2.im, vv, Z4));
    }
#pragma unroll
    for (int r = 0; r < 4; ++r) ST64S((u32x2{o[0][r] & ~1u, o[1][r]}), rws, toff, r * 262144, AUX_SC1);
  }

  PRIO(0);
  const unsigned eps = __builtin_amdgcn_readfirstlane(ep), tag = mktag(eps);
  MEMBAR();
#define tailp ((const u32x4*)((const char*)((lane & 16) ? P.wi : P.wr) + ((wrow_of(idx) * 1026) & ~15) + 1024))
#define taild (lds + L_W + (q & 1) * (16 * WRS) + WROW(idx) + 1024)
  if (W_MODE == 0) load_filter(w);
  if (w < 8) {
    const int e = (0x83783378 >> (4 * w)) & 15, h = (0x91 >> w) & 1, c = ((0x6A >> w) & 1) << (e - 1), d = w == C_GQ;
    const bool swap = w >= C_M1C, tw = (0x4C >> w) & 1;
    const float unit = ((0xA0 >> w) & 1) ? -1.f / 2048 : 1.f / 2048;
    u32x4 c0, c1;
#pragma unroll
    for (int j = 0; j < 4; ++j) {
      const int s = 4 * q + j, x = swap ? idx : s, y = swap ? s : idx;
      const int m = (((x >> h) << e) + c) * (y + d) + ((h & x & (y >> 3)) << 10);
      const float rev = (float)(m & 2047) * unit;
      const float cs = cosr(rev), sn = sinr(rev);
      unsigned p0 = pack2(cs, sn), p1 = pack2(-sn, cs);
      PIN(p0); PIN(p1);
      c0[j] = tw ? __float_as_uint(cs) : p0;
      c1[j] = tw ? __float_as_uint(sn) : p1;
    }
    *(u32x4*)(cl + w * 2048) = c0;
    *(u32x4*)(cl + w * 2048 + 1024) = c1;
  }
  const int kp = 4 * (w & 3) + (idx >> 2) + 64 * (w >> 2) + 16 * (idx & 3);
  const int kf = kp + 256 * (q >> 1);
  float ss, cc, sc;
  {
    const float nrm = 1.f / (2048.f * 512.f);
    const float th = (float)kf * (1.f / 1024);
    const float sn = sinr(th) * (0.5f * nrm);
    ss = 0.5f * nrm - sn; cc = 0.5f * nrm + sn; sc = cosr(th) * (0.5f * nrm);
  }
  const int krow = special ? 128 * hb : (hb ? 256 - b : b);
  unsigned poff = ((krow * 8 + w8) * 2048 + (64 * q + idx) * 8) ^ (T_SWZ * (krow & 12) * 128);
  const int pj = T_SWZ * (krow & 3);
  int wa = hb * (256 * XS) + (idx + 64 * q) * XS + w8 * 8;
  int rp = kp * XS + q * 16, rp8 = rp + 8;
  int pp = idx * IS + (4 * (w & 3) + q) * 64 + ((w >> 2) ^ ((0x78 >> (2 * (w & 3))) & 3)) * 16;
  int rd = w * (2 * IS) + idx * 64 + (q ^ ((0x78 >> (2 * (idx >> 2))) & 3)) * 16;
  unsigned wp1 = lb + L_W + (q & 1) * WROW(4) + 2 * (b & 7) + 2 * kf, wp2 = lb + L_W + WROW(8) + (q & 1) * WROW(4) + 2 * (-b & 7) + 1024 - 2 * kf;
  auto patch_dc = [&](int rowp, int rowq, int lp, int lq) {
    char* pa = lds + L_W + ((lane >> 4) & 1) * (16 * WRS) + ((lane >> 3) & 1) * 1024;
    char* p1 = pa + WROW(rowp) + lp; char* p2 = pa + WROW(rowq) + lq;
    const float x1 = ldbf(p1), x2 = ldbf(p2), sg = (lane & 16) ? -0.5f : 0.5f;
    MEMBAR();
    *(uint16_t*)p1 = (uint16_t)pack2(0.5f * x1 + sg * x2, 0.f);
    *(uint16_t*)p2 = (uint16_t)pack2(0.5f * x2 + sg * x1, 0.f);
    MEMBAR();
  };
  PIN(ss); PIN(cc); PIN(sc);
  PIN(wa); PIN(poff); PIN(rp); PIN(rp8); PIN(pp); PIN(rd); PIN(wp1); PIN(wp2);
  Coef cf;
  auto filter_setup = [&]() {
    if (special) {
      if (w == 0) { const int k2 = lane & 7; patch_dc(lane < 32 ? k2 : 8 + k2, lane < 32 ? (8 - k2) & 7 : 15 - k2, 0, 0); }
      const unsigned m0 = wp2 - WROW(8), m1 = lb + L_W + (1 - (q & 1)) * WROW(4) + 1024 - 2 * kf;
      const Coef c2 = mkcoef(read_filter<true>(wp1 + WROW(8), m1 + WROW(8) + 3 * WRS, m1 + WROW(8)), ss, cc, sc);
      *(u32x4*)(lds + L_SV + threadIdx.x * 16) = pack4(c2.br, c2.bi);
      cf = mkcoef(read_filter<true>(wp1, m0, m1 + WRS), ss, cc, sc);
      cf.gr = c2.ar; cf.gi = c2.ai;
    } else {
      if (w == 0) patch_dc(lane & 7, 8 + (lane & 7), 2 * (b & 7), 2 * (-b & 7));
      cf = mkcoef(read_filter<false>(wp1, wp2, 0), ss, cc, sc);
    }
    PIN(cf.ar); PIN(cf.ai); PIN(cf.gr); PIN(cf.gi); PIN(cf.br); PIN(cf.bi);
  };
  const unsigned foff = WS_F1 + (b % NREP) * REP_STRIDE + (w8 * 16 + idx) * 4;
  auto poll = [&]() {
    int spins = 0;
    for (;;) {
      MEMBAR();
      unsigned f = LD32(rws, foff, AUX_SC1);
      if (__all(f == tag) || ++spins > SPIN_CAP) break;
      __builtin_amdgcn_s_sleep(POLL_SLEEP);
    }
    MEMBAR();
  };
  if (W_MODE == 0) {
    if (w == 8) {
      const u32x4 t = __builtin_nontemporal_load(tailp);
      asm volatile("s_waitcnt vmcnt(3)" ::: "memory");
      __builtin_amdgcn_s_barrier();
      *(u32x4*)taild = t;
    } else {
      asm volatile("s_waitcnt vmcnt(2)" ::: "memory");
      __builtin_amdgcn_s_barrier();
    }
    MEMBAR();
    if (threadIdx.x < NREP) ST32(tag, rws, WS_F1 + threadIdx.x * REP_STRIDE + (n2 * 16 + cg) * 4, AUX_SC1);

    if (w == 0) asm volatile("s_waitcnt vmcnt(1)" ::: "memory"); else asm volatile("s_waitcnt vmcnt(0)" ::: "memory");
    LDSBAR();

    filter_setup();

    poll();
  } else {
    asm volatile("s_waitcnt vmcnt(0)" ::: "memory");
    __builtin_amdgcn_s_barrier();
    MEMBAR();

    if (hb) {
      if (w == 8 && lane < NREP) ST32(tag, rws, WS_F1 + lane * REP_STRIDE + (n2 * 16 + cg) * 4, AUX_SC1);
      MEMBAR();
      load_filter(w); load_filter(w - 8);
    } else {
      if (w == 1) *(u32x4*)taild = __builtin_nontemporal_load(tailp);
      __builtin_amdgcn_s_sleep(PRE_SLEEP);
      poll();
    }

    __builtin_amdgcn_s_barrier();
    MEMBAR();
  }

  CFrag M1, G, M1c;
  f32x4 twc[3], tws[3];
  {
    u32x2 d[4];
#pragma unroll
    for (int j = 0; j < 4; ++j) d[j] = LD64S(rws, poff, (j ^ pj) * 128, AUX_SC1);
    MEMBAR();
    M1 = ldfrag(cl + C_M1 * 2048); G = ldfrag(cl + C_G * 2048);
#pragma unroll
  for (int c = 0; c < 2; ++c) { twc[c] = *(const f32x4*)(cl + (C_TW0 + c) * 2048); tws[c] = *(const f32x4*)(cl + (C_TW0 + c) * 2048 + 1024); }
    PINF(M1); PINF(G); PIN(twc[0]); PIN(twc[1]); PIN(tws[0]); PIN(tws[1]);

    SCHED();
    PRIO(3);
    u32x4 o[2];
#pragma unroll
    for (int c = 0; c < 2; ++c) {
      u32x4 a = {d[0][c], d[1][c], d[2][c], d[3][c]};
      f32x4 dr = mfma16(a, F.re, Z4), di = mfma16(a, F.im, Z4);
      u32x4 bb = pack4(dr * twc[c] + di * tws[c], di * twc[c] - dr * tws[c]);
      o[c] = pack4(mfma16(c ? M1.re : F.re, bb, Z4), mfma16(c ? M1.im : F.im, bb, Z4));
    }
#pragma unroll
    for (int r = 0; r < 4; ++r) st8(lds + L_R1 + wa + 16 * r * XS, u32x2{o[0][r], o[1][r]});
  }
  CFrag Fc = conjfrag(F), Gc = ldfrag(cl + C_GC * 2048);
  PINF(Fc);

  LDSBAR();
  if (W_MODE == 3) { filter_setup(); }
  {
    f32x4 ar[2], ai[2];
#pragma unroll
    for (int tt = 0; tt < 2; ++tt) {
      u32x4 dd = ld16(lds + L_R1 + tt * (256 * XS) + rp, lds + L_R1 + tt * (256 * XS) + rp8);
      ar[tt] = mfma16(G.re, dd, Z4); ai[tt] = mfma16(G.im, dd, Z4);
    }
    u32x4 y[2];
    if (!special) {
      y[0] = pack4(cf.ar * ar[0] - cf.ai * ai[0] + cf.br * ar[1] - cf.bi * ai[1], cf.ar * ai[0] + cf.ai * ar[0] + cf.br * ai[1] + cf.bi * ar[1]);
      y[1] = pack4(cf.gr * ar[1] - cf.gi * ai[1] - cf.br * ar[0] + cf.bi * ai[0], cf.gr * ai[1] + cf.gi * ar[1] - cf.br * ai[0] - cf.bi * ar[0]);
    } else {
      const char* mp = lds + L_R1 + ((256 - kp) & 255) * XS + q * 16;
      const unsigned m1 = kp == 0 ? 0x80000000u : 0x00008000u;
      const u32x4 mk = {0x80000000u, m1, 0x80000000u, m1};
      const u32x4 d0 = ld16(mp, mp + 8) ^ mk, d1 = ld16(mp + 256 * XS, mp + 256 * XS + 8) ^ mk;
      const CFrag Gq = ldfrag(cl + C_GQ * 2048);
      const u32x4 sv = *(const u32x4*)(lds + L_SV + threadIdx.x * 16);
      const f32x4 b2r = {bflo(sv[0]), bflo(sv[1]), bflo(sv[2]), bflo(sv[3])}, b2i = {bfhi(sv[0]), bfhi(sv[1]), bfhi(sv[2]), bfhi(sv[3])};
      f32x4 br = mfma16(G.re, d0, Z4), bi = mfma16(G.im, d0, Z4);
      y[0] = pack4(cf.ar * ar[0] - cf.ai * ai[0] + cf.br * br - cf.bi * bi, cf.ar * ai[0] + cf.ai * ar[0] + cf.br * bi + cf.bi * br);
      br = mfma16(Gq.re, d1, Z4); bi = mfma16(Gq.im, d1, Z4);
      y[1] = pack4(cf.gr * ar[1] - cf.gi * ai[1] + b2r * br - b2i * bi, cf.gr * ai[1] + cf.gi * ar[1] + b2r * bi + b2i * br);
    }
    MEMBAR();
    M1c = ldfrag(cl + C_M1C * 2048);
    twc[2] = *(const f32x4*)(cl + C_TW1C * 2048); tws[2] = *(const f32x4*)(cl + C_TW1C * 2048 + 1024);
#pragma unroll
    for (int tt = 0; tt < 2; ++tt) *(u32x4*)(lds + L_R0 + tt * (16 * IS) + pp) = pack4(mfma16(y[tt], Gc.re, Z4), mfma16(y[tt], Gc.im, Z4));
  }
  LDSBAR();
  {
    u32x4 o[2];
#pragma unroll
    for (int c = 0; c < 2; ++c) {
      const u32x4 a = *(const u32x4*)(lds + L_R0 + rd + c * IS);
      f32x4 dr = mfma16(a, c ? M1c.re : Fc.re, Z4), di = mfma16(a, c ? M1c.im : Fc.im, Z4);
      u32x4 bb = pack4(dr * twc[2 * c] - di * tws[2 * c], di * twc[2 * c] + dr * tws[2 * c]);
      o[c] = pack4(mfma16(Fc.re, bb, Z4), mfma16(Fc.im, bb, Z4));
    }
#pragma unroll
    for (int r = 0; r < 4; ++r) ST64S((u32x2{o[0][r] | 1u, o[1][r]}), rws, poff, (r ^ pj) * 128, AUX_SC1);
  }

  PRIO(0);
  {
    CFrag A3;
#pragma unroll
    for (int j = 0; j < 4; ++j) {
      const int k1 = w + 16 * (4 * q + j);
      setfrag<-1>(A3, j, (float)((8 * idx * k1 + (k1 > 128 ? k1 - 256 : k1) * n2) & 2047) * (1.f / 2048));
      const unsigned sg = k1 > 128 ? 0x80000000u : 0u;
      A3.re[j] ^= sg; A3.im[j] ^= sg;
    }
    unsigned ooff = ((64 * q + w) * 8 + n2) * 4096 + 2048 + cg * 128 + idx * 8;
    PINF(A3); PIN(ooff);
    u32x2 d[4];
    int spins = 0;
    if (POLL2_PRE == 1) asm volatile("s_waitcnt vmcnt(0)" ::: "memory");
    if (POLL2_PRE == 2) { asm volatile("s_waitcnt vmcnt(0)" ::: "memory"); __builtin_amdgcn_s_barrier(); }
    if (POLL2_PRE == 3) __builtin_amdgcn_s_barrier();

    for (;;) {
      MEMBAR();
#pragma unroll
      for (int j = 0; j < 4; ++j) d[j] = LD64S(rws, toff, j * 262144, AUX_SC1);
      const unsigned all = d[0][0] & d[1][0] & d[2][0] & d[3][0];
      if (__all(all & 1u) || ++spins > SPIN_CAP) break;
      __builtin_amdgcn_s_sleep(POLL2_SLEEP);
    }

    PRIO(3);
    u32x4 o[2];
#pragma unroll
    for (int tt = 0; tt < 2; ++tt) {
      u32x4 dd = {d[0][tt], d[1][tt], d[2][tt], d[3][tt]};
      o[tt] = pack4(mfma16(A3.re, dd, Z4), mfma16(A3.im, dd, Z4));
    }
#pragma unroll
    for (int r = 0; r < 4; ++r) st8(lds + L_R1 + ewr + r * ES, u32x2{o[0][r], o[1][r]});
    LDSBAR();
    if (b == 1 && threadIdx.x == 0) ST32(eps + 1, rws, WS_EP, AUX_SC1);
    u32x2 e[4];
#pragma unroll
    for (int j = 0; j < 4; ++j) e[j] = ld8(lds + L_R1 + erd + j * 128);
#pragma unroll
    for (int tt = 0; tt < 2; ++tt) {
      u32x4 vv = {e[0][tt], e[1][tt], e[2][tt], e[3][tt]};
      o[tt] = pack4(mfma16(Fc.re, vv, Z4), mfma16(Fc.im, vv, Z4));
    }
    const rsrc_t ro = mkrsrc(P.out, 2048 * 2048 * 2);
#pragma unroll
    for (int r = 0; r < 4; ++r) ST64S((u32x2{o[0][r], o[1][r]}), ro, ooff, r * 524288, OUT_AUX);
  }

}

template <int V> struct IC { static constexpr int value = V; };
template <int I, int N, class Fn>
DI void static_for(Fn&& fn) { if constexpr (I < N) { fn(IC<I>{}); static_for<I + 1, N>(fn); } }
DI f32x2 cmul(f32x2 a, f32x2 z) {
  f32x2 t, d;
  asm("v_pk_mul_f32 %0, %1, %2 op_sel_hi:[0,1]" : "=v"(t) : "v"(a), "v"(z));
  asm("v_pk_fma_f32 %0, %1, %2, %3 op_sel:[1,1,0] op_sel_hi:[1,0,1] neg_lo:[0,1,0]" : "=v"(d) : "v"(a), "v"(z), "v"(t));
  return d;
}
DI f32x2 cmulc(f32x2 a, f32x2 z) {
  f32x2 t, d;
  asm("v_pk_mul_f32 %0, %1, %2 op_sel_hi:[0,1] neg_hi:[0,1]" : "=v"(t) : "v"(a), "v"(z));
  asm("v_pk_fma_f32 %0, %1, %2, %3 op_sel:[1,1,0] op_sel_hi:[1,0,1]" : "=v"(d) : "v"(a), "v"(z), "v"(t));
  return d;
}
DI f32x2 csq(f32x2 a) { return cmul(a, a); }
DI f32x2 cpow2(float lr2, float rh, float rl, float e) {
  float m = __builtin_amdgcn_exp2f(lr2 * e);
  float fr = __builtin_fmaf(rl, e, __builtin_amdgcn_fractf(rh * e));
  return f32x2{cosr(fr), sinr(fr)} * m;
}
DI float exp_acc(float x) {
  const float t = x * 1.44269504088896341f;
  const float e = __builtin_fmaf(x, 1.44269504088896341f, -t) + x * 1.925963033500011e-08f;
  const float r = __builtin_amdgcn_exp2f(t);
  return __builtin_fmaf(r, e * 0.693147180559945f, r);
}
template <int O, int S>
DI void strow4(unsigned base, unsigned a, unsigned b, unsigned c, unsigned d) {
  asm volatile("s_mov_b32 m0, %4\n\ts_nop 0\n\tds_write_addtid_b32 %0 offset:%5\n\tds_write_addtid_b32 %1 offset:%6\n\t"
               "ds_write_addtid_b32 %2 offset:%7\n\tds_write_addtid_b32 %3 offset:%8"
               :: "v"(a), "v"(b), "v"(c), "v"(d), "s"(base), "i"(O), "i"(O + S), "i"(O + 2 * S), "i"(O + 3 * S) : "memory");
}
template <int O>
DI void strow1(unsigned base, unsigned a) {
  asm volatile("s_mov_b32 m0, %1\n\ts_nop 0\n\tds_write_addtid_b32 %0 offset:%2" :: "v"(a), "s"(base), "i"(O) : "memory");
}
#define Y_PRIO 2
#define W_PF 0
#define PF_AUX 2
#define U_ROT 2
#define U_P0 0
#define U_P1 2
#define U_AUX 0
#define PD 4

template <int K>
DI void store_if(u32x4 v, unsigned off, rsrc_t r, int slot) {
  asm volatile("s_cmp_lg_u32 %3, %4\n\ts_cbranch_scc1 .Lskip%=\n\tbuffer_store_dwordx4 %0, %1, %2, 0 offen\n\ts_nop 1\n.Lskip%=:"
               :: "v"(v), "v"(off), "s"(r), "s"(slot), "i"(K) : "scc");
}

DI void role_ssm(const Params& P, char* lds, const int a) {
  char* ws = P.ws; (void)ws;
  const int d0 = 8 * ((a & 7) * 16 + (a >> 3));
  const int tid = threadIdx.x, lane = tid & 63, w = __builtin_amdgcn_readfirstlane(tid >> 6), idx = lane & 15, q = lane >> 4;
  const int ch = w & 7, hf = w >> 3;

  const rsrc_t ru = mkrsrc(P.u, 2048 * 1024 * 2);
  const rsrc_t ro = mkrsrc(P.out, 2048 * 2048 * 2);
  const int pn = (d0 + ch) * 128 + hf * 64 + lane;
  unsigned pa = __builtin_nontemporal_load(P.logA + pn), pi = __builtin_nontemporal_load(P.Aim + pn), pr = __builtin_nontemporal_load(P.Cr + pn),
           pc = __builtin_nontemporal_load(P.Ci + pn), pd = P.logdt[d0 + ch], pk = P.Dskip[d0 + ch];
  u32x4 uv[2];
  const int wr = (w + U_ROT * (a >> 3)) & 15;
  const unsigned uoff = (128 * wr + 2 * lane) * 2048 + d0 * 2;
  auto load_u = [&](int r) { MEMBAR(); uv[r] = LD128(ru, uoff + r * 2048, U_AUX); MEMBAR(); };
#pragma unroll
  for (int j = 0; j < 3; ++j) if (j < 2 || tid < (8 * A_KS - 32768) / 16) *(u32x4*)(lds + A_K + j * 16384 + tid * 16) = u32x4{0, 0, 0, 0};
  if (hf) {
    char* z = lds + A_UL + ch * A_ULS;
    *(u32x2*)(z + lane * 8) = u32x2{0, 0};
    if (lane < 4) *(u32x2*)(z + (lane == 0 ? 512 : 4608 + lane * 8)) = u32x2{0, 0};
  } else if (w < 6) {
    const int lo = (0x209072 >> (4 * w)) & 15, hi = (0x46F896 >> (4 * w)) & 15;
    const unsigned bits = (((2u << (hi - lo)) - 1u) << (idx + lo)) >> (8 * q);
    const unsigned val = w >= 4 ? 0xffffffffu : 0x3f803f80u;
    u32x4 f;
#pragma unroll
    for (int j = 0; j < 4; ++j)
      f[j] = plo((unsigned)__builtin_amdgcn_sbfe((int)bits, 2 * j, 1), (unsigned)__builtin_amdgcn_sbfe((int)bits, 2 * j + 1, 1)) & val;
    *(u32x4*)(lds + A_M + w * 1024 + lane * 16) = f;
  }
  if (U_P0 < 0) { __builtin_amdgcn_s_barrier(); load_u(0); }
  if (U_P1 < 0) load_u(1);
  PIN(pa); PIN(pi); PIN(pr); PIN(pc); PIN(pd); PIN(pk);
  u32x4 pf0, pf1;
  auto prefetch_w = [&]() {
    const int r8 = 256 * (w & 7);
    const int row = a == 0 ? 128 * hf + r8 : (hf ? 2048 - a - r8 : a + r8);
    const unsigned off = (row * 1026) & ~15;
    MEMBAR();
    pf0 = __builtin_amdgcn_raw_buffer_load_b128(mkrsrc(P.wr, 2048 * 513 * 2), lane * 16 + (lane == 63 ? 16 : 0), off, PF_AUX);
    pf1 = __builtin_amdgcn_raw_buffer_load_b128(mkrsrc(P.wi, 2048 * 513 * 2), lane * 16 + (lane == 63 ? 16 : 0), off, PF_AUX);
    MEMBAR();
  };
  if (W_PF == 1) prefetch_w();
  if (U_P0 == 0) load_u(0);
  if (W_PF == 2) prefetch_w();
  if (U_P1 == 0) load_u(1);
  char* st = lds + w * A_STS;
  const unsigned stb = __builtin_amdgcn_readfirstlane((unsigned)(size_t)st);
  f32x4 D[2][2];
  {
    const float dt = rbf(exp_acc(bflo(pd)));
    const float lr2 = rbf(exp_acc(bflo(pa))) * dt * -1.44269504088896341f;
    const float th = bflo(pi) * dt;

    const float ihi = 0.15915494309189535f, ilo = 6.4206382e-09f;
    const float rh = th * ihi, rl = __builtin_fmaf(th, ilo, __builtin_fmaf(th, ihi, -rh));
    const f32x2 z1 = cpow2(lr2, rh, rl, 1.f), z2 = csq(z1), z8 = csq(csq(z2));
    const f32x2 c32 = cpow2(lr2, rh, rl, 32.f);
    const char* sr = st + idx * 272 + q * 16;
    u32x4 Bf[2][4];
    f32x2 c4[4];
    c4[1] = z8; c4[2] = csq(z8); c4[3] = cmul(c4[2], z8);
    strow4<0, 272>(stb, 0x3f80u, pack2v(c4[1]), pack2v(c4[2]), pack2v(c4[3]));
    c4[0] = c32; c4[1] = cmul(c4[1], c32); c4[2] = cmul(c4[2], c32); c4[3] = cmul(c4[3], c32);
#define BSTEP(S, MORE) do { strow4<4 * S * 272, 272>(stb, pack2v(c4[0]), pack2v(c4[1]), pack2v(c4[2]), pack2v(c4[3])); \
      if (MORE) { c4[0] = cmul(c4[0], c32); c4[1] = cmul(c4[1], c32); c4[2] = cmul(c4[2], c32); c4[3] = cmul(c4[3], c32); } } while (0)
    BSTEP(1, 1); BSTEP(2, 1); BSTEP(3, 1);
#pragma unroll
    for (int ks = 0; ks < 4; ++ks) Bf[0][ks] = *(const u32x4*)(sr + ks * 64);
    const f32x2 z256 = csq(c4[0]);
    if (U_P0 == 1) load_u(0);
    if (U_P1 == 1) load_u(1);
    BSTEP(0, 1); BSTEP(1, 1); BSTEP(2, 1); BSTEP(3, 0);
#pragma unroll
    for (int ks = 0; ks < 4; ++ks) Bf[1][ks] = *(const u32x4*)(sr + ks * 64);

    if (U_P0 == 2) load_u(0);
    if (U_P1 == 2) load_u(1);
    const f32x2 c1 = z1, c2 = z2, c256 = z256, c512 = csq(z256);
    const f32x2 C = f32x2{bflo(pr), -bflo(pc)} * (dt + dt);
    f32x2 a0 = C, a1 = cmulc(C, c1), s0 = cmulc(C, c256);
    f32x2 a2 = cmulc(a0, c2), a3 = cmulc(a1, c2), s1 = cmulc(s0, c2);
    strow4<0, 272>(stb, pack2v(a0), pack2v(a1), pack2v(a2), pack2v(a3));
    a0 = cmulc(a2, c2); a1 = cmulc(a3, c2); f32x2 s2 = cmulc(s1, c2);
    a2 = cmulc(a0, c2); a3 = cmulc(a1, c2); f32x2 s3 = cmulc(s2, c2);
    strow4<4 * 272, 272>(stb, pack2v(a0), pack2v(a1), pack2v(a2), pack2v(a3));
    f32x2 s4 = cmulc(s3, c2), t516 = cmulc(s2, c256), t772 = cmulc(s2, c512);
    strow4<8 * 272, 272>(stb, pack2v(s0), pack2v(s1), pack2v(s2), pack2v(s3));
    strow1<12 * 272>(stb, pack2v(s4));
    const int rw0 = (int)((0x889AAABC01234567ull >> (4 * idx)) & 15);
    const char* p0 = st + rw0 * 272 + q * 16;
    u32x4 f0[4];
#pragma unroll
    for (int ks = 0; ks < 4; ++ks) f0[ks] = *(const u32x4*)(p0 + ks * 64);
    f32x2 t520 = cmulc(s4, c256), t776 = cmulc(s4, c512);
    strow4<0, 272>(stb, pack2v(t520), pack2v(t516), pack2v(t776), pack2v(t772));
    f32x2 e0 = cmulc(t520, c512), e1 = cmulc(t776, c512);
    f32x2 e2 = cmulc(e0, c512), e3 = cmulc(e1, c512);
    strow4<4 * 272, 272>(stb, pack2v(e0), pack2v(e1), pack2v(e2), pack2v(e3));
    const char* p1 = st + (idx & 7) * 272 + q * 16;
#pragma unroll
    for (int tb = 0; tb < 2; ++tb) { D[0][tb] = Z4; D[1][tb] = Z4; }
#pragma unroll
    for (int ks = 0; ks < 4; ++ks) {
      u32x4 f1 = *(const u32x4*)(p1 + ks * 64);
#pragma unroll
      for (int tb = 0; tb < 2; ++tb) { D[0][tb] = mfma16(f0[ks], Bf[tb][ks], D[0][tb]); D[1][tb] = mfma16(f1, Bf[tb][ks], D[1][tb]); }
    }
  }
  if (U_P0 == 3) load_u(0);
  if (U_P1 == 3) load_u(1);
  char* kk = lds + A_K + ch * A_KS;
  const char* ul = lds + A_UL + ch * A_ULS;
  const char* ub = ul + 512 + 32 * idx + 16 * q;
  const char* ml = lds + A_M + lane * 16;
  unsigned ka[2], kc[2];
  {
    if (hf) {
      *(f32x4*)(st + lane * 16) = D[0][0]; *(f32x4*)(st + 1024 + lane * 16) = D[0][1];
      D[0][0] = D[1][0]; D[0][1] = D[1][1];
    } else {
      *(f32x4*)(st + lane * 16) = D[1][0]; *(f32x4*)(st + 1024 + lane * 16) = D[1][1];
      D[0][0][3] += lane == 16 ? bflo(pk) : 0.f;
    }

    if (W_PF) asm volatile("" :: "v"(pf0), "v"(pf1));
    const unsigned base = __builtin_amdgcn_readfirstlane((unsigned)(size_t)lds + 40000u + 256u * wr);
    constexpr int O = A_UL + 520 - 40000;
    strow4<O, A_ULS>(base, plo(uv[0][0], uv[1][0]), phi(uv[0][0], uv[1][0]), plo(uv[0][1], uv[1][1]), phi(uv[0][1], uv[1][1]));
    strow4<O + 4 * A_ULS, A_ULS>(base, plo(uv[0][2], uv[1][2]), phi(uv[0][2], uv[1][2]), plo(uv[0][3], uv[1][3]), phi(uv[0][3], uv[1][3]));
    LDSBAR();

    if (hf) __builtin_amdgcn_s_setprio(Y_PRIO);
    const char* ps = lds + (w ^ 8) * A_STS + lane * 16;
#pragma unroll
    for (int tb = 0; tb < 2; ++tb) {
      f32x4 k = D[0][tb] + *(const f32x4*)(ps + tb * 1024);
      ka[tb] = pack2(k[0], k[1]); kc[tb] = pack2(k[2], k[3]);
      PIN(ka[tb]); PIN(kc[tb]);
    }
  }
  f32x4 acc[8];
#pragma unroll
  for (int t = 0; t < 8; ++t) acc[t] = Z4;
  if (hf == 0) {
    u32x4 H[17];
    {
      int xw = 2 * (531 - 7 - 128 - 4 - 256 * (q >> 1) - 8 * idx + 4 * (q & 1));
      PIN(xw);
#pragma unroll
      for (int tb = 0; tb < 2; ++tb) {
        char* p = kk + xw + 8 + 256 * (1 - tb);
        const unsigned mid = __builtin_amdgcn_alignbit(kc[tb], ka[tb], 16);
        *(u32x2*)p = u32x2{ka[tb], kc[tb]};
        *(unsigned*)(p + 2 * A_CS - 4) = ka[tb]; *(unsigned*)(p + 2 * A_CS) = kc[tb];
#pragma unroll
        for (int sft = 1; sft < 4; sft += 2) {
          char* po = p + sft * A_CS - 2 * sft;
          *(uint16_t*)po = (uint16_t)ka[tb]; *(unsigned*)(po + 2) = mid; *(uint16_t*)(po + 6) = (uint16_t)(kc[tb] >> 16);
        }
      }
      const int x0 = 527 - 512 - idx + 8 * q;
      int hp = (x0 & 3) * A_CS + (x0 & ~3) * 2, hp8 = hp + 8;
      PIN(hp); PIN(hp8);
      const u32x4 m511 = *(const u32x4*)(ml + 5120);
#pragma unroll
      for (int i = 0; i < 17; ++i) {
        const int m = i == 16 ? 0 : ((i & 1) ? 8 - (i >> 1) : 16 - (i >> 1));
        H[m] = ld16(kk + hp + 64 * (16 - m), kk + hp8 + 64 * (16 - m));
      }
      const unsigned k511 = __builtin_amdgcn_readlane(ka[1], 47) & 0xffffu;
      H[16] |= m511 & (k511 * 0x10001u);
    }

    u32x4 ring[PD + 1];
#pragma unroll
    for (int i = 0; i < PD; ++i) ring[i] = *(const u32x4*)(ub + 64 * (i - 8));
    SCHED();
    f32x4 part;
    const char* pt = lds + A_PT + ch * A_STS + lane * 16;
    char* yw = lds + (8 + ch) * A_STS + (ch & 3) * 32 + 32 * idx + 8 * q;
    static_for<-8, 57>([&](auto fc) {
      constexpr int f = decltype(fc)::value;
      if constexpr (f + PD <= 56) ring[(f + 8 + PD) % (PD + 1)] = *(const u32x4*)(ub + 64 * (f + PD));
      if constexpr ((f & 7) == 6 && f >= 14) {
        constexpr int al = (f + 2) / 8;
        part = *(const f32x4*)(pt + ((al >> 1) & 1) * (8 * A_STS) + (al & 1) * 1024);
      }
      static_for<0, 8>([&](auto ac) {
        constexpr int al = decltype(ac)::value, m = 8 * al - f;
        if constexpr (m >= 0 && m <= 16) acc[al] = mfma16(H[m], ring[(f + 8) % (PD + 1)], acc[al]);
      });
      if constexpr ((f > 0 && (f & 7) == 1) || f == 56) {
        constexpr int al = f / 8;
        f32x4 y = acc[al];
        if constexpr (al >= 2) y += part;
        *(u32x2*)(yw + (al & 3) * 512) = u32x2{pack2(y[0], y[1]), pack2(y[2], y[3])};
        if constexpr (al & 1) { LDSBAR(); }
      }
      SCHED();
    });
  } else {
    u32x4 T2[6], T3[4], M[5];
    {
      const unsigned k1024 = __builtin_amdgcn_readlane(kc[1], 15) & 0xffffu;
      const unsigned sa = q >= 2 ? ka[1] : ka[0];
      unsigned sc = q >= 2 ? kc[1] : kc[0];
      sc = lane == 47 ? sc & 0xffff0000u : sc;
      const int nb = 31 - 16 * (q >> 1) - idx;
      int a0 = (q & 1) ? A_R3 + 2 * (32 + nb) : A_R2 + 4 * (32 + nb);
      int a1 = a0 + ((q & 1) ? -64 : 2);
      PIN(a0); PIN(a1);
      *(uint16_t*)(kk + a0 + 128) = (uint16_t)sa; *(uint16_t*)(kk + a1 + 128) = (uint16_t)(sa >> 16);
      *(uint16_t*)(kk + a0) = (uint16_t)sc; *(uint16_t*)(kk + a1) = (uint16_t)(sc >> 16);
      int t2 = A_R2 + 8 * (15 - idx) + 16 * q, t3 = A_R3 + 4 * (15 - idx) + 16 * q, t28 = t2 + 8;
      PIN(t2); PIN(t3); PIN(t28);
#pragma unroll
      for (int i = 0; i < 5; ++i) M[i] = *(const u32x4*)(ml + 1024 * i);
#pragma unroll
      for (int dl = 3; dl <= 8; ++dl) T2[dl - 3] = ld16(kk + t2 + 64 * (8 - dl), kk + t28 + 64 * (8 - dl));
#pragma unroll
      for (int dl = 4; dl <= 7; ++dl) {
        const unsigned* p = (const unsigned*)(kk + t3 + 64 * (7 - dl));
        T3[dl - 4] = u32x4{p[0], p[1], p[2], p[3]};
      }
      M[4] &= k1024 * 0x10001u;
    }

    const char* wc = ul + 480 + 32 * idx + 16 * q;
    const char* wb = ul + 480 + 16 * idx + 16 * q;
    const char* yr = lds + 8 * A_STS + (idx >> 2) * (A_STS + 32) + (64 * ch + 16 * q + 4 * (idx & 3)) * 2;
    const unsigned eo = (64 * ch + lane) * 4096 + d0 * 2;
    const int slot = ch & 3;
#pragma unroll
    for (int c = 0; c < 5; ++c) {
      u32x4 ev;
      if (c > 0) {
        u32x2 e0 = trread(yr + ((c - 1) & 1) * 1024), e1 = trread(yr + ((c - 1) & 1) * 1024 + 4 * A_STS);
        ev = u32x4{e0[0], e0[1], e1[0], e1[1]};
      }
      auto emit = [&](auto kc_) { if (c > 0) store_if<decltype(kc_)::value>(ev, eo + (c - 1) * (512 * 4096), ro, slot); };
      if (c >= 3) ST128(ev, ro, eo + (c - 1) * (512 * 4096), 0);
      if (c < 3) {
        u32x4 wn[4], w3[2], uh[2];
#pragma unroll
        for (int i = 0; i < 4; ++i) wn[i] = *(const u32x4*)(wb + 256 * (4 * c + i));
#pragma unroll
        for (int i = 0; i < 2; ++i) {
          if (c > 0) w3[i] = *(const u32x4*)(wc + 512 * (2 * c - 2 + i));
          if (2 * c + 2 + i >= 3) uh[i] = *(const u32x4*)(ub + 64 * (8 * (2 * c + 2 + i) - 32));
        }
        u32x4 v[4], v3[2];
        emit(IC<0>{});
        SCHED();
#pragma unroll
        for (int i = 0; i < 4; ++i) v[i] = pack4(mfma16(wn[i], M[0], Z4), mfma16(wn[i], M[1], Z4));
        SCHED();
        emit(IC<1>{});
        if (c > 0) {
#pragma unroll
          for (int i = 0; i < 2; ++i) v3[i] = pack4(mfma16(w3[i], M[2], Z4), mfma16(w3[i], M[3], Z4));
        }
#pragma unroll
        for (int i = 0; i < 2; ++i) if (2 * c + 2 + i >= 3) acc[2 * c + 2 + i] = mfma16(M[4], uh[i], acc[2 * c + 2 + i]);
        SCHED();
        emit(IC<2>{});
#pragma unroll
        for (int i = 0; i < 4; ++i)
#pragma unroll
          for (int al = 2; al < 8; ++al) {
            const int dl = 2 * al - (4 * c + i);
            if (dl >= 3 && dl <= 8) acc[al] = mfma16(v[i], T2[dl - 3], acc[al]);
          }
        SCHED();
        emit(IC<3>{});
        if (c > 0) {
#pragma unroll
          for (int i = 0; i < 2; ++i)
#pragma unroll
            for (int al = 2 * c + 2 + i; al < 8; ++al) acc[al] = mfma16(v3[i], T3[al - (2 * c - 2 + i) - 4], acc[al]);
        }
        char* pt = lds + A_PT + ((c + 1) & 1) * (8 * A_STS) + ch * A_STS + lane * 16;
        *(f32x4*)pt = acc[2 * c + 2];
        *(f32x4*)(pt + 1024) = acc[2 * c + 3];
      }
      if (c < 4) { LDSBAR(); }
    }
  }

}

#if defined(__HIP_DEVICE_COMPILE__)
#define NO_LS_OPT __attribute__((target("no-load-store-opt")))
#else
#define NO_LS_OPT
#endif
__global__ __launch_bounds__(1024, 4) NO_LS_OPT void fused(Params P) {
  extern __shared__ __attribute__((aligned(16))) char lds[];
  const int b = blockIdx.x;
  if (b < 128) { if (ROLE_MASK & 1) role_spectral(P, lds, b); }
  else { if (ROLE_MASK & 2) role_ssm(P, lds, b - 128); }
}

extern "C" void kernel_launch(void* const* d_in, const int* in_sizes, int n_in,
                              void* d_out, int out_size, void* d_ws, size_t ws_size,
                              hipStream_t stream) {
  Params P;
  P.u = (const uint16_t*)d_in[0]; P.logA = (const uint16_t*)d_in[1]; P.Aim = (const uint16_t*)d_in[2]; P.Cr = (const uint16_t*)d_in[3];
  P.Ci = (const uint16_t*)d_in[4]; P.logdt = (const uint16_t*)d_in[5]; P.Dskip = (const uint16_t*)d_in[6]; P.x = (const uint16_t*)d_in[7];
  P.wr = (const uint16_t*)d_in[8]; P.wi = (const uint16_t*)d_in[9]; P.out = (uint16_t*)d_out; P.ws = (char*)d_ws;
  (void)hipFuncSetAttribute((const void*)fused, hipFuncAttributeMaxDynamicSharedMemorySize, LDS_BYTES);
  fused<<<256, 1024, LDS_BYTES, stream>>>(P);
}
```

```cpp
#include <hip/hip_runtime.h>
#include <hip/hip_bf16.h>
#include <stdint.h>

typedef __attribute__((ext_vector_type(8))) __bf16 bf16x8;
typedef __attribute__((ext_vector_type(2))) __bf16 bf16x2;
typedef __attribute__((ext_vector_type(2))) float f32x2;
typedef __attribute__((ext_vector_type(4))) float f32x4;
typedef __attribute__((ext_vector_type(4))) unsigned u32x4;
typedef __attribute__((ext_vector_type(2))) unsigned u32x2;
typedef __attribute__((ext_vector_type(4))) short s16x4;
#define LAS __attribute__((address_space(3)))
#define DI __device__ __forceinline__

#define ROLE_MASK 3

#define MEMBAR() asm volatile("" ::: "memory")
#define PIN(x) asm volatile("" : "+v"(x))
#define PINF(f) do { PIN((f).re); PIN((f).im); } while (0)
#define SCHED() __builtin_amdgcn_sched_barrier(0)
#define DRAIN() asm volatile("s_waitcnt vmcnt(0)" ::: "memory")
#define AUX_NT 0
#define DMA_NT ""
#define AUX_SC1 16
typedef __amdgpu_buffer_rsrc_t rsrc_t;
DI rsrc_t mkrsrc(const void* p, unsigned bytes) { return __builtin_amdgcn_make_buffer_rsrc((void*)p, 0, bytes, 0x00020000); }
#define LD64S(r, off, soff, aux) __builtin_amdgcn_raw_buffer_load_b64(r, off, soff, aux)
#define ST64S(v, r, off, soff, aux) __builtin_amdgcn_raw_buffer_store_b64(v, r, off, soff, aux)
#define LDSBAR() do { asm volatile("s_waitcnt lgkmcnt(0)" ::: "memory"); __builtin_amdgcn_s_barrier(); MEMBAR(); } while (0)
#define LD32(r, off, aux) __builtin_amdgcn_raw_buffer_load_b32(r, off, 0, aux)
#define LD64(r, off, aux) __builtin_amdgcn_raw_buffer_load_b64(r, off, 0, aux)
#define LD128(r, off, aux) __builtin_amdgcn_raw_buffer_load_b128(r, off, 0, aux)
#define ST32(v, r, off, aux) __builtin_amdgcn_raw_buffer_store_b32(v, r, off, 0, aux)
#define ST64(v, r, off, aux) __builtin_amdgcn_raw_buffer_store_b64(v, r, off, 0, aux)
#define ST128(v, r, off, aux) __builtin_amdgcn_raw_buffer_store_b128(v, r, off, 0, aux)

DI unsigned pack2(float a, float b) { f32x2 v = {a, b}; bf16x2 r = __builtin_convertvector(v, bf16x2); return __builtin_bit_cast(unsigned, r); }
DI unsigned pack2v(f32x2 v) { bf16x2 r = __builtin_convertvector(v, bf16x2); return __builtin_bit_cast(unsigned, r); }
DI f32x4 mfma16(u32x4 a, u32x4 b, f32x4 c) {
  return __builtin_amdgcn_mfma_f32_16x16x32_bf16(__builtin_bit_cast(bf16x8, a), __builtin_bit_cast(bf16x8, b), c, 0, 0, 0);
}
DI float bflo(unsigned v) { return __uint_as_float(v << 16); }
DI float bfhi(unsigned v) { return __uint_as_float(v & 0xffff0000u); }
DI float rbf(float x) { return bflo(pack2(x, 0.f)); }
DI float cosr(float rev) { return __builtin_amdgcn_cosf(rev); }
DI float sinr(float rev) { return __builtin_amdgcn_sinf(rev); }
DI float mirror16(float v) { return __builtin_bit_cast(float, __builtin_amdgcn_update_dpp(0, __builtin_bit_cast(int, v), 0x140, 0xf, 0xf, true)); }
DI f32x4 mirror4(f32x4 v) { return f32x4{mirror16(v[0]), mirror16(v[1]), mirror16(v[2]), mirror16(v[3])}; }
DI u32x4 pack4(f32x4 re, f32x4 im) { return u32x4{pack2(re[0], im[0]), pack2(re[1], im[1]), pack2(re[2], im[2]), pack2(re[3], im[3])}; }
DI unsigned plo(unsigned a, unsigned b) { return __builtin_amdgcn_perm(b, a, 0x05040100u); }
DI unsigned phi(unsigned a, unsigned b) { return __builtin_amdgcn_perm(b, a, 0x07060302u); }
static constexpr f32x4 Z4 = {0.f, 0.f, 0.f, 0.f};

struct CFrag { u32x4 re, im; };
template <int SGN>
DI void setfrag(CFrag& f, int j, float rev) {
  float c = cosr(rev), s = sinr(rev);
  if (SGN > 0) { f.re[j] = pack2(c, s); f.im[j] = pack2(-s, c); }
  else { f.re[j] = pack2(c, -s); f.im[j] = pack2(s, c); }
}
DI void setrot(CFrag& f, int j, float rev, unsigned im) {
  const unsigned p = pack2(cosr(rev), sinr(rev));
  f.re[j] = p; f.im[j] = __builtin_amdgcn_alignbit(p, p, 16) ^ im;
}
DI CFrag conjfrag(const CFrag& f) {
  CFrag g;
#pragma unroll
  for (int j = 0; j < 4; ++j) { g.re[j] = f.re[j] ^ 0x80000000u; g.im[j] = f.im[j] ^ 0x00008000u; }
  return g;
}

#define WS_T 0u
#define WS_F1 (4u << 20)
#define WS_EP (WS_F1 + 786432u)
#define WS_BYTES (5u << 20)
#define SETUP_FIRST 0
#define USE_PRIO 1
#define PRIO(n) do { if (USE_PRIO) __builtin_amdgcn_s_setprio(n); } while (0)
#define POLL2_SLEEP 2
#define POLL2_PRE 0
#define EARLY_POLL 0
#define W_MODE 0
#define PRE_SLEEP 2
#define W_LATE 0
#define W_POS 2
#define T_SWZ 1
#define POLL_MODE 0
#define NREP 32
#define REP_STRIDE 512
#define POLL_SLEEP 1
#define SPIN_CAP (1 << 18)
#define OUT_AUX 16
DI unsigned mktag(unsigned e) { return (e ^ 0x5bd1e995u) * 0x9E3779B1u + 0x7F4A7C15u; }

#define XS 72
#define ES 2560
#define L_R0 0
#define L_R1 40960
#define L_W 81920
#define WRS 1056
#define W_EX (32 * WRS)
#define L_C (L_W + 33920)
#define L_SV (L_C + 16384)
#define A_STS 4352
#define A_UL (16 * A_STS)
#define A_ULS 4640
#define A_K (A_UL + 8 * A_ULS)
#define A_CS 1216
#define A_R2 (4 * A_CS)
#define A_R3 (A_R2 + 512)
#define A_KS 5696
#define A_M (A_K + 8 * A_KS)
#define A_PT 2176
#define LDS_BYTES (A_M + 6144)

struct Params {
  const uint16_t *u, *logA, *Aim, *Cr, *Ci, *logdt, *Dskip, *x, *wr, *wi;
  uint16_t* out;
  char* ws;
};
DI u32x2 ld8(const char* p) { return *(const u32x2*)p; }
DI void st8(char* p, u32x2 v) { *(u32x2*)p = v; }
DI u32x4 ld16(const char* p0, const char* p1) { const u32x2 lo = ld8(p0), hi = ld8(p1); return u32x4{lo[0], lo[1], hi[0], hi[1]}; }
DI u32x4 xor4(u32x4 a, unsigned m) { return a ^ u32x4{m, m, m, m}; }
DI unsigned mirru(unsigned v) { return (unsigned)__builtin_amdgcn_update_dpp(0, (int)v, 0x140, 0xf, 0xf, true); }
DI f32x4 unpk4(u32x2 v) { return f32x4{bflo(v[0]), bfhi(v[0]), bflo(v[1]), bfhi(v[1])}; }
DI u32x2 trread(const char* p) { return __builtin_bit_cast(u32x2, __builtin_amdgcn_ds_read_tr16_b64_v4i16((LAS s16x4*)p)); }
DI CFrag ldfrag(const char* p) { CFrag f; f.re = *(const u32x4*)p; f.im = *(const u32x4*)(p + 1024); return f; }

enum { C_GQ = 0, C_M1, C_TW0, C_TW1, C_G, C_M1C, C_TW1C, C_GC };

DI void dma16(rsrc_t r, unsigned voff, unsigned soff, unsigned ldsaddr) {
  asm volatile("s_mov_b32 m0, %3\n\ts_nop 0\n\tbuffer_load_dwordx4 %0, %1, %2 offen" DMA_NT " lds" :: "v"(voff), "s"(r), "s"(soff), "s"(ldsaddr) : "memory");
}
struct WSet { f32x4 r[2], i[2]; };
DI float ldbf(const char* p) { return bflo(*(const uint16_t*)p); }
static_assert(WRS == 1056, "offsets below");
template <bool MIRROR>
DI WSet read_filter(unsigned p1, unsigned p2, unsigned p3) {
  float t[16];
  if (MIRROR)
    asm volatile("ds_read_u16_d16_hi %0, %16 offset:0\n\tds_read_u16_d16_hi %1, %16 offset:1056\n\tds_read_u16_d16_hi %2, %16 offset:2112\n\tds_read_u16_d16_hi %3, %16 offset:3168\n\tds_read_u16_d16_hi %4, %17 offset:0\n\tds_read_u16_d16_hi %5, %18 offset:2112\n\tds_read_u16_d16_hi %6, %18 offset:1056\n\tds_read_u16_d16_hi %7, %18 offset:0\n\tds_read_u16_d16_hi %8, %16 offset:16896\n\tds_read_u16_d16_hi %9, %16 offset:17952\n\tds_read_u16_d16_hi %10, %16 offset:19008\n\tds_read_u16_d16_hi %11, %16 offset:20064\n\tds_read_u16_d16_hi %12, %17 offset:16896\n\tds_read_u16_d16_hi %13, %18 offset:19008\n\tds_read_u16_d16_hi %14, %18 offset:17952\n\tds_read_u16_d16_hi %15, %18 offset:16896\n\ts_waitcnt lgkmcnt(0)"
                 : "=&v"(t[0]), "=&v"(t[1]), "=&v"(t[2]), "=&v"(t[3]), "=&v"(t[4]), "=&v"(t[5]), "=&v"(t[6]), "=&v"(t[7]), "=&v"(t[8]), "=&v"(t[9]), "=&v"(t[10]), "=&v"(t[11]), "=&v"(t[12]), "=&v"(t[13]), "=&v"(t[14]), "=&v"(t[15]) : "v"(p1), "v"(p2), "v"(p3) : "memory");
  else
    asm volatile("ds_read_u16_d16_hi %0, %16 offset:0\n\tds_read_u16_d16_hi %1, %16 offset:1056\n\tds_read_u16_d16_hi %2, %16 offset:2112\n\tds_read_u16_d16_hi %3, %16 offset:3168\n\tds_read_u16_d16_hi %4, %17 offset:0\n\tds_read_u16_d16_hi %5, %17 offset:1056\n\tds_read_u16_d16_hi %6, %17 offset:2112\n\tds_read_u16_d16_hi %7, %17 offset:3168\n\tds_read_u16_d16_hi %8, %16 offset:16896\n\tds_read_u16_d16_hi %9, %16 offset:17952\n\tds_read_u16_d16_hi %10, %16 offset:19008\n\tds_read_u16_d16_hi %11, %16 offset:20064\n\tds_read_u16_d16_hi %12, %17 offset:16896\n\tds_read_u16_d16_hi %13, %17 offset:17952\n\tds_read_u16_d16_hi %14, %17 offset:19008\n\tds_read_u16_d16_hi %15, %17 offset:20064\n\ts_waitcnt lgkmcnt(0)"
                 : "=&v"(t[0]), "=&v"(t[1]), "=&v"(t[2]), "=&v"(t[3]), "=&v"(t[4]), "=&v"(t[5]), "=&v"(t[6]), "=&v"(t[7]), "=&v"(t[8]), "=&v"(t[9]), "=&v"(t[10]), "=&v"(t[11]), "=&v"(t[12]), "=&v"(t[13]), "=&v"(t[14]), "=&v"(t[15]) : "v"(p1), "v"(p2) : "memory");
  WSet s;
  s.r[0] = f32x4{t[0], t[1], t[2], t[3]}; s.r[1] = f32x4{t[4], t[5], t[6], t[7]};
  s.i[0] = f32x4{t[8], t[9], t[10], t[11]}; s.i[1] = f32x4{t[12], t[13], t[14], t[15]};
  return s;
}
struct Coef { f32x4 ar, ai, gr, gi, br, bi; };
DI Coef mkcoef(const WSet& s, float ss, float cc, float sc) {
  Coef c;
  c.ar = s.r[0] * ss + s.r[1] * cc; c.ai = s.i[0] * ss - s.i[1] * cc;
  c.gr = s.r[0] * cc + s.r[1] * ss; c.gi = s.i[0] * cc - s.i[1] * ss;
  c.br = (s.i[1] + s.i[0]) * -sc; c.bi = (s.r[0] - s.r[1]) * sc;
  return c;
}
#define WROW(r) ((r) * WRS + ((r) & 4) * 4)
#define IS 1040

DI void role_spectral(const Params& P, char* lds, const int b) {
  char* ws = P.ws;
  const int lane = threadIdx.x & 63, w = __builtin_amdgcn_readfirstlane(threadIdx.x >> 6), idx = lane & 15, q = lane >> 4;
  const int hb = w >> 3, w8 = w & 7;

  const rsrc_t rws = mkrsrc(ws, WS_BYTES);
  const rsrc_t rx = mkrsrc(P.x, 2048 * 1024 * 2);
  const rsrc_t rwr = mkrsrc(P.wr, 2048 * 513 * 2);
  const rsrc_t rwi = mkrsrc(P.wi, 2048 * 513 * 2);
  const int n2 = b & 7, cg = b >> 3;
  const bool special = b == 0;
  const unsigned lb = __builtin_amdgcn_readfirstlane((unsigned)(size_t)lds);
  auto wrow_of = [&](int r) { return special ? 128 * (r >> 3) + 256 * (r & 7) : ((r >> 3) ? 2048 - b - 256 * (r & 7) : b + 256 * (r & 7)); };
  auto load_filter = [&](int r) {
    const unsigned off = (wrow_of(r) * 1026) & ~15;
    const unsigned dst = lb + L_W + WROW(r);
    dma16(rwr, lane * 16, off, dst);
    dma16(rwi, lane * 16, off, dst + 16 * WRS);
  };
#define tailp ((const u32x4*)((const char*)((lane & 16) ? P.wi : P.wr) + ((wrow_of(idx) * 1026) & ~15) + 1024))
#define taild (lds + L_W + (q & 1) * (16 * WRS) + WROW(idx) + 1024)
  const unsigned xoff = ((64 * q + w) * 8 + n2) * 2048 + cg * 128 + idx * 8;
  unsigned toff = xoff ^ (T_SWZ * w * 128);
  u32x2 v[4];
#pragma unroll
  for (int j = 0; j < 4; ++j) v[j] = LD64S(rx, xoff, j * 262144, AUX_NT);
  const unsigned ep = LD32(rws, WS_EP, AUX_SC1);
  u32x2 v4chk = v[3];
  if (W_MODE == 2) {
    MEMBAR();
    load_filter(w);
  }
  (void)v4chk;
  char* cl = lds + L_C + lane * 16;
  CFrag F;
  const float fidx = (float)idx, fq = (float)(4 * q);
  {
    const float d = fidx * (1.f / 16), r0 = d * fq;
#pragma unroll
    for (int j = 0; j < 4; ++j) setrot(F, j, r0 + (float)j * d, 0x8000u);
  }
  int ewr = (4 * q) * ES + (w + (w >> 2)) * 128 + idx * 8;
  int erd = w * ES + (5 * q) * 128 + idx * 8;
  CFrag A2;
  {
    const bool up = w + 16 * idx > 128;
    const float d = fidx * (1.f / 16) + (float)w * (1.f / 256);
    const float r0 = d * (fq + (float)n2 * 0.125f) - (up ? (float)n2 * 0.125f : 0.f);
    const unsigned im = up ? 0x80000000u : 0x8000u;
#pragma unroll
    for (int j = 0; j < 4; ++j) setrot(A2, j, r0 + (float)j * d, im);
  }
  PINF(A2); PIN(ewr); PIN(erd); PIN(toff);
  {
    PINF(F);
    SCHED();

    PRIO(3);
    u32x4 o[2];
#pragma unroll
    for (int tt = 0; tt < 2; ++tt) {
      u32x4 dd = {v[0][tt], v[1][tt], v[2][tt], v[3][tt]};
      o[tt] = pack4(mfma16(F.re, dd, Z4), mfma16(F.im, dd, Z4));
    }
#pragma unroll
    for (int r = 0; r < 4; ++r) st8(lds + L_R0 + ewr + r * ES, u32x2{o[0][r], o[1][r]});

    if (W_MODE == 1) load_filter(w);
    LDSBAR();
    u32x2 e[4];
#pragma unroll
    for (int j = 0; j < 4; ++j) e[j] = ld8(lds + L_R0 + erd + j * 128);
#pragma unroll
    for (int tt = 0; tt < 2; ++tt) {
      u32x4 vv = {e[0][tt], e[1][tt], e[2][tt], e[3][tt]};
      o[tt] = pack4(mfma16(A2.re, vv, Z4), mfma16(A2.im, vv, Z4));
    }
#pragma unroll
    for (int r = 0; r < 4; ++r) ST64S((u32x2{o[0][r] & ~1u, o[1][r]}), rws, toff, r * 262144, AUX_SC1);
  }

  PRIO(0);
  const unsigned eps = __builtin_amdgcn_readfirstlane(ep), tag = mktag(eps);
  MEMBAR();
  if (W_MODE == 0) load_filter(w);
  if (w < 8) {
    const int e = (0x83783378 >> (4 * w)) & 15, h = (0x91 >> w) & 1, c = ((0x6A >> w) & 1) << (e - 1), d = w == C_GQ;
    const bool swap = w >= C_M1C, tw = (0x4C >> w) & 1;
    const float unit = ((0xA0 >> w) & 1) ? -1.f / 2048 : 1.f / 2048;
    u32x4 c0, c1;
#pragma unroll
    for (int j = 0; j < 4; ++j) {
      const int s = 4 * q + j, x = swap ? idx : s, y = swap ? s : idx;
      const int m = (((x >> h) << e) + c) * (y + d) + ((h & x & (y >> 3)) << 10);
      const float rev = (float)(m & 2047) * unit;
      const float cs = cosr(rev), sn = sinr(rev);
      unsigned p0 = pack2(cs, sn), p1 = pack2(-sn, cs);
      PIN(p0); PIN(p1);
      c0[j] = tw ? __float_as_uint(cs) : p0;
      c1[j] = tw ? __float_as_uint(sn) : p1;
    }
    *(u32x4*)(cl + w * 2048) = c0;
    *(u32x4*)(cl + w * 2048 + 1024) = c1;
    if (special && (w & 3) == 0) {
      const u32x4 mk = {0x80000000u, 0x8000u, 0x80000000u, 0x8000u};
      *(u32x4*)(cl + 16384 + w * 512) = c0 ^ mk;
      *(u32x4*)(cl + 16384 + w * 512 + 1024) = c1 ^ mk;
    }
  }
  const int kp = 4 * (w & 3) + (idx >> 2) + 64 * (w >> 2) + 16 * (idx & 3);
  const int kf = kp + 256 * (q >> 1);
  float ss, cc, sc;
  {
    const float nrm = 1.f / (2048.f * 512.f);
    const float th = (float)kf * (1.f / 1024);
    const float sn = sinr(th) * (0.5f * nrm);
    ss = 0.5f * nrm - sn; cc = 0.5f * nrm + sn; sc = cosr(th) * (0.5f * nrm);
  }
  const int krow = special ? 128 * hb : (hb ? 256 - b : b);
  unsigned poff = ((krow * 8 + w8) * 2048 + (64 * q + idx) * 8) ^ (T_SWZ * (krow & 12) * 128);
  const int pj = T_SWZ * (krow & 3);
  int wa = hb * (256 * XS) + (idx + 64 * q) * XS + w8 * 8;
  int rp = kp * XS + q * 16, rp8 = rp + 8;
  int pp = idx * IS + (4 * (w & 3) + q) * 64 + ((w >> 2) ^ ((0x78 >> (2 * (w & 3))) & 3)) * 16;
  int rd = w * (2 * IS) + idx * 64 + (q ^ ((0x78 >> (2 * (idx >> 2))) & 3)) * 16;
  unsigned wp1 = lb + L_W + (q & 1) * WROW(4) + 2 * (b & 7) + 2 * kf, wp2 = lb + L_W + WROW(8) + (q & 1) * WROW(4) + 2 * (-b & 7) + 1024 - 2 * kf;
  auto patch_dc = [&](int rowp, int rowq, int lp, int lq) {
    char* pa = lds + L_W + ((lane >> 4) & 1) * (16 * WRS) + ((lane >> 3) & 1) * 1024;
    char* p1 = pa + WROW(rowp) + lp; char* p2 = pa + WROW(rowq) + lq;
    const float x1 = ldbf(p1), x2 = ldbf(p2), sg = (lane & 16) ? -0.5f : 0.5f;
    MEMBAR();
    *(uint16_t*)p1 = (uint16_t)pack2(0.5f * x1 + sg * x2, 0.f);
    *(uint16_t*)p2 = (uint16_t)pack2(0.5f * x2 + sg * x1, 0.f);
    MEMBAR();
  };
  PIN(ss); PIN(cc); PIN(sc);
  PIN(wa); PIN(poff); PIN(rp); PIN(rp8); PIN(pp); PIN(rd); PIN(wp1); PIN(wp2);
  Coef cf;
  f32x4 b2r, b2i;
  auto filter_setup = [&]() {
    if (special) {
      if (w == 0) { const int k2 = lane & 7; patch_dc(lane < 32 ? k2 : 8 + k2, lane < 32 ? (8 - k2) & 7 : 15 - k2, 0, 0); }
      const unsigned m0 = wp2 - WROW(8), m1 = lb + L_W + (1 - (q & 1)) * WROW(4) + 1024 - 2 * kf;
      cf = mkcoef(read_filter<true>(wp1, m0, m1 + WRS), ss, cc, sc);
      const Coef c2 = mkcoef(read_filter<true>(wp1 + WROW(8), m1 + WROW(8) + 3 * WRS, m1 + WROW(8)), ss, cc, sc);
      b2r = c2.br; b2i = c2.bi;
      PIN(b2r); PIN(b2i);
      wp1 = ((256 - kp) & 255) * XS + q * 16; wp2 = wp1 + 8;
      PIN(wp1); PIN(wp2);
      cf.gr = c2.ar; cf.gi = c2.ai;
    } else {
      if (w == 0) patch_dc(lane & 7, 8 + (lane & 7), 2 * (b & 7), 2 * (-b & 7));
      cf = mkcoef(read_filter<false>(wp1, wp2, 0), ss, cc, sc);
    }
    PIN(cf.ar); PIN(cf.ai); PIN(cf.gr); PIN(cf.gi); PIN(cf.br); PIN(cf.bi);
  };
  const unsigned foff = WS_F1 + (b % NREP) * REP_STRIDE + (w8 * 16 + idx) * 4;
  auto poll = [&]() {
    int spins = 0;
    for (;;) {
      MEMBAR();
      unsigned f = LD32(rws, foff, AUX_SC1);
      if (__all(f == tag) || ++spins > SPIN_CAP) break;
      __builtin_amdgcn_s_sleep(POLL_SLEEP);
    }
    MEMBAR();
  };
  if (W_MODE == 1 || W_MODE == 2) {
    if (w == 8) *(u32x4*)taild = *tailp;
    if (SETUP_FIRST) {
      asm volatile("s_waitcnt vmcnt(4)" ::: "memory");
      LDSBAR();
      filter_setup();
    }
    asm volatile("s_waitcnt vmcnt(0) lgkmcnt(0)" ::: "memory");
    __builtin_amdgcn_s_barrier();
    MEMBAR();
    if (threadIdx.x < NREP) ST32(tag, rws, WS_F1 + threadIdx.x * REP_STRIDE + (n2 * 16 + cg) * 4, AUX_SC1);

    if (!SETUP_FIRST) filter_setup();

    __builtin_amdgcn_s_sleep(PRE_SLEEP);
    poll();
  } else if (W_MODE == 0) {
    if (w == 8) {
      const u32x4 t = *tailp;
      asm volatile("s_waitcnt vmcnt(3)" ::: "memory");
      __builtin_amdgcn_s_barrier();
      *(u32x4*)taild = t;
    } else {
      asm volatile("s_waitcnt vmcnt(2)" ::: "memory");
      __builtin_amdgcn_s_barrier();
    }
    MEMBAR();
    if (threadIdx.x < NREP) ST32(tag, rws, WS_F1 + threadIdx.x * REP_STRIDE + (n2 * 16 + cg) * 4, AUX_SC1);

    if (w == 0) asm volatile("s_waitcnt vmcnt(1)" ::: "memory"); else asm volatile("s_waitcnt vmcnt(0)" ::: "memory");
    LDSBAR();

    if (EARLY_POLL) {
      unsigned f0 = LD32(rws, foff, AUX_SC1);
      MEMBAR();
      filter_setup();

      if (!__all(f0 == tag)) poll();
      MEMBAR();
    } else {
      filter_setup();

      poll();
    }
  } else {
    asm volatile("s_waitcnt vmcnt(0)" ::: "memory");
    __builtin_amdgcn_s_barrier();
    MEMBAR();

    if (hb) {
      if (w == 8 && lane < NREP) ST32(tag, rws, WS_F1 + lane * REP_STRIDE + (n2 * 16 + cg) * 4, AUX_SC1);
      MEMBAR();
      load_filter(w); load_filter(w - 8);
    } else {
      if (w == 1) *(u32x4*)taild = *tailp;
      __builtin_amdgcn_s_sleep(PRE_SLEEP);
      poll();
    }

    __builtin_amdgcn_s_barrier();
    MEMBAR();
  }

  CFrag M1, G, M1c;
  f32x4 twc[3], tws[3];
  {
    u32x2 d[4];
#pragma unroll
    for (int j = 0; j < 4; ++j) d[j] = LD64S(rws, poff, (j ^ pj) * 128, AUX_SC1);
    MEMBAR();
    M1 = ldfrag(cl + C_M1 * 2048); G = ldfrag(cl + C_G * 2048);
#pragma unroll
  for (int c = 0; c < 2; ++c) { twc[c] = *(const f32x4*)(cl + (C_TW0 + c) * 2048); tws[c] = *(const f32x4*)(cl + (C_TW0 + c) * 2048 + 1024); }
    PINF(M1); PINF(G); PIN(twc[0]); PIN(twc[1]); PIN(tws[0]); PIN(tws[1]);

    SCHED();
    PRIO(3);
    u32x4 o[2];
#pragma unroll
    for (int c = 0; c < 2; ++c) {
      u32x4 a = {d[0][c], d[1][c], d[2][c], d[3][c]};
      f32x4 dr = mfma16(a, F.re, Z4), di = mfma16(a, F.im, Z4);
      u32x4 bb = pack4(dr * twc[c] + di * tws[c], di * twc[c] - dr * tws[c]);
      o[c] = pack4(mfma16(c ? M1.re : F.re, bb, Z4), mfma16(c ? M1.im : F.im, bb, Z4));
    }
#pragma unroll
    for (int r = 0; r < 4; ++r) st8(lds + L_R1 + wa + 16 * r * XS, u32x2{o[0][r], o[1][r]});
  }
  CFrag Fc = conjfrag(F), Gc = ldfrag(cl + C_GC * 2048);
  PINF(Fc);

  LDSBAR();
  if (W_MODE == 3) { filter_setup(); }
  {
    f32x4 ar[2], ai[2];
#pragma unroll
    for (int tt = 0; tt < 2; ++tt) {
      u32x4 dd = ld16(lds + L_R1 + tt * (256 * XS) + rp, lds + L_R1 + tt * (256 * XS) + rp8);
      ar[tt] = mfma16(G.re, dd, Z4); ai[tt] = mfma16(G.im, dd, Z4);
    }
    u32x4 y[2];
    if (!special) {
      y[0] = pack4(cf.ar * ar[0] - cf.ai * ai[0] + cf.br * ar[1] - cf.bi * ai[1], cf.ar * ai[0] + cf.ai * ar[0] + cf.br * ai[1] + cf.bi * ar[1]);
      y[1] = pack4(cf.gr * ar[1] - cf.gi * ai[1] - cf.br * ar[0] + cf.bi * ai[0], cf.gr * ai[1] + cf.gi * ar[1] - cf.br * ai[0] - cf.bi * ar[0]);
    } else {
      u32x4 d0 = ld16(lds + L_R1 + wp1, lds + L_R1 + wp2), d1 = ld16(lds + L_R1 + 256 * XS + wp1, lds + L_R1 + 256 * XS + wp2);
      const CFrag Gm = ldfrag(cl + 16384 + 2048), Gq = ldfrag(cl + 16384);
      if (w == 0) { const unsigned m = idx == 0 ? 0x80008000u : 0u; d0[1] ^= m; d0[3] ^= m; d1[1] ^= m; d1[3] ^= m; }
      f32x4 br = mfma16(Gm.re, d0, Z4), bi = mfma16(Gm.im, d0, Z4);
      y[0] = pack4(cf.ar * ar[0] - cf.ai * ai[0] + cf.br * br - cf.bi * bi, cf.ar * ai[0] + cf.ai * ar[0] + cf.br * bi + cf.bi * br);
      br = mfma16(Gq.re, d1, Z4); bi = mfma16(Gq.im, d1, Z4);
      y[1] = pack4(cf.gr * ar[1] - cf.gi * ai[1] + b2r * br - b2i * bi, cf.gr * ai[1] + cf.gi * ar[1] + b2r * bi + b2i * br);
    }
    MEMBAR();
    M1c = ldfrag(cl + C_M1C * 2048);
    twc[2] = *(const f32x4*)(cl + C_TW1C * 2048); tws[2] = *(const f32x4*)(cl + C_TW1C * 2048 + 1024);
#pragma unroll
    for (int tt = 0; tt < 2; ++tt) *(u32x4*)(lds + L_R0 + tt * (16 * IS) + pp) = pack4(mfma16(y[tt], Gc.re, Z4), mfma16(y[tt], Gc.im, Z4));
  }
  LDSBAR();
  {
    u32x4 o[2];
#pragma unroll
    for (int c = 0; c < 2; ++c) {
      const u32x4 a = *(const u32x4*)(lds + L_R0 + rd + c * IS);
      f32x4 dr = mfma16(a, c ? M1c.re : Fc.re, Z4), di = mfma16(a, c ? M1c.im : Fc.im, Z4);
      u32x4 bb = pack4(dr * twc[2 * c] - di * tws[2 * c], di * twc[2 * c] + dr * tws[2 * c]);
      o[c] = pack4(mfma16(Fc.re, bb, Z4), mfma16(Fc.im, bb, Z4));
    }
#pragma unroll
    for (int r = 0; r < 4; ++r) ST64S((u32x2{o[0][r] | 1u, o[1][r]}), rws, poff, (r ^ pj) * 128, AUX_SC1);
  }

  PRIO(0);
  {
    CFrag A3;
    {
      int idx3 = idx, q3 = q;
      PIN(idx3); PIN(q3);
      const float g = (float)(8 * idx3 + n2) * (1.f / 2048), d = g * 16.f, r0 = g * (float)(w + 64 * q3), sh = (float)n2 * 0.125f;
#pragma unroll
      for (int j = 0; j < 4; ++j) {
        const bool up = j == 0 ? w + 64 * q3 > 128 : q3 >= 2;
        const float rev = r0 + (float)j * d;
        setrot(A3, j, up ? rev - sh : -rev, up ? 0x80000000u : 0x8000u);
      }
    }
    unsigned ooff = ((64 * q + w) * 8 + n2) * 4096 + 2048 + cg * 128 + idx * 8;
    PINF(A3); PIN(ooff);
    u32x2 d[4];
    int spins = 0;
    if (POLL2_PRE == 1) asm volatile("s_waitcnt vmcnt(0)" ::: "memory");
    if (POLL2_PRE == 2) { asm volatile("s_waitcnt vmcnt(0)" ::: "memory"); __builtin_amdgcn_s_barrier(); }
    if (POLL2_PRE == 3) __builtin_amdgcn_s_barrier();

    for (;;) {
      MEMBAR();
#pragma unroll
      for (int j = 0; j < 4; ++j) d[j] = LD64S(rws, toff, j * 262144, AUX_SC1);
      const unsigned all = d[0][0] & d[1][0] & d[2][0] & d[3][0];
      if (__all(all & 1u) || ++spins > SPIN_CAP) break;
      __builtin_amdgcn_s_sleep(POLL2_SLEEP);
    }

    PRIO(3);
    u32x4 o[2];
#pragma unroll
    for (int tt = 0; tt < 2; ++tt) {
      u32x4 dd = {d[0][tt], d[1][tt], d[2][tt], d[3][tt]};
      o[tt] = pack4(mfma16(A3.re, dd, Z4), mfma16(A3.im, dd, Z4));
    }
#pragma unroll
    for (int r = 0; r < 4; ++r) st8(lds + L_R1 + ewr + r * ES, u32x2{o[0][r], o[1][r]});
    LDSBAR();
    if (b == 1 && threadIdx.x == 0) ST32(eps + 1, rws, WS_EP, AUX_SC1);
    u32x2 e[4];
#pragma unroll
    for (int j = 0; j < 4; ++j) e[j] = ld8(lds + L_R1 + erd + j * 128);
#pragma unroll
    for (int tt = 0; tt < 2; ++tt) {
      u32x4 vv = {e[0][tt], e[1][tt], e[2][tt], e[3][tt]};
      o[tt] = pack4(mfma16(Fc.re, vv, Z4), mfma16(Fc.im, vv, Z4));
    }
    const rsrc_t ro = mkrsrc(P.out, 2048 * 2048 * 2);
#pragma unroll
    for (int r = 0; r < 4; ++r) ST64S((u32x2{o[0][r], o[1][r]}), ro, ooff, r * 524288, OUT_AUX);
  }

}

template <int V> struct IC { static constexpr int value = V; };
template <int I, int N, class Fn>
DI void static_for(Fn&& fn) { if constexpr (I < N) { fn(IC<I>{}); static_for<I + 1, N>(fn); } }
DI f32x2 cmul(f32x2 a, f32x2 z) {
  f32x2 t, d;
  asm("v_pk_mul_f32 %0, %1, %2 op_sel_hi:[0,1]" : "=v"(t) : "v"(a), "v"(z));
  asm("v_pk_fma_f32 %0, %1, %2, %3 op_sel:[1,1,0] op_sel_hi:[1,0,1] neg_lo:[0,1,0]" : "=v"(d) : "v"(a), "v"(z), "v"(t));
  return d;
}
DI f32x2 cmulc(f32x2 a, f32x2 z) {
  f32x2 t, d;
  asm("v_pk_mul_f32 %0, %1, %2 op_sel_hi:[0,1] neg_hi:[0,1]" : "=v"(t) : "v"(a), "v"(z));
  asm("v_pk_fma_f32 %0, %1, %2, %3 op_sel:[1,1,0] op_sel_hi:[1,0,1]" : "=v"(d) : "v"(a), "v"(z), "v"(t));
  return d;
}
DI f32x2 csq(f32x2 a) { return cmul(a, a); }
DI f32x2 cpow2(float lr2, float rh, float rl, float e) {
  float m = __builtin_amdgcn_exp2f(lr2 * e);
  float fr = __builtin_fmaf(rl, e, __builtin_amdgcn_fractf(rh * e));
  return f32x2{cosr(fr), sinr(fr)} * m;
}
DI float exp_acc(float x) {
  const float t = x * 1.44269504088896341f;
  const float e = __builtin_fmaf(x, 1.44269504088896341f, -t) + x * 1.925963033500011e-08f;
  const float r = __builtin_amdgcn_exp2f(t);
  return __builtin_fmaf(r, e * 0.693147180559945f, r);
}
template <int O, int S>
DI void strow4(unsigned base, unsigned a, unsigned b, unsigned c, unsigned d) {
  asm volatile("s_mov_b32 m0, %4\n\ts_nop 0\n\tds_write_addtid_b32 %0 offset:%5\n\tds_write_addtid_b32 %1 offset:%6\n\t"
               "ds_write_addtid_b32 %2 offset:%7\n\tds_write_addtid_b32 %3 offset:%8"
               :: "v"(a), "v"(b), "v"(c), "v"(d), "s"(base), "i"(O), "i"(O + S), "i"(O + 2 * S), "i"(O + 3 * S) : "memory");
}
template <int O>
DI void strow1(unsigned base, unsigned a) {
  asm volatile("s_mov_b32 m0, %1\n\ts_nop 0\n\tds_write_addtid_b32 %0 offset:%2" :: "v"(a), "s"(base), "i"(O) : "memory");
}
#define Y_PRIO 2
#define W_PF 0
#define PF_AUX 0
#define U_ROT 2
#define U_P0 0
#define U_P1 2
#define U_AUX 0
#define PD 4

template <int K>
DI void store_if(u32x4 v, unsigned off, rsrc_t r, int slot) {
  asm volatile("s_cmp_lg_u32 %3, %4\n\ts_cbranch_scc1 .Lskip%=\n\tbuffer_store_dwordx4 %0, %1, %2, 0 offen\n\ts_nop 1\n.Lskip%=:"
               :: "v"(v), "v"(off), "s"(r), "s"(slot), "i"(K) : "scc");
}

DI void role_ssm(const Params& P, char* lds, const int a) {
  char* ws = P.ws; (void)ws;
  const int d0 = 8 * ((a & 7) * 16 + (a >> 3));
  const int tid = threadIdx.x, lane = tid & 63, w = __builtin_amdgcn_readfirstlane(tid >> 6), idx = lane & 15, q = lane >> 4;
  const int ch = w & 7, hf = w >> 3;

  const rsrc_t ru = mkrsrc(P.u, 2048 * 1024 * 2);
  const rsrc_t ro = mkrsrc(P.out, 2048 * 2048 * 2);
  const int pn = (d0 + ch) * 128 + hf * 64 + lane;
  unsigned pa = P.logA[pn], pi = P.Aim[pn], pr = P.Cr[pn],
           pc = P.Ci[pn], pd = P.logdt[d0 + ch], pk = P.Dskip[d0 + ch];
  u32x4 uv[2];
  const int wr = (w + U_ROT * (a >> 3)) & 15;
  const unsigned uoff = (128 * wr + 2 * lane) * 2048 + d0 * 2;
  auto load_u = [&](int r) { MEMBAR(); uv[r] = LD128(ru, uoff + r * 2048, U_AUX); MEMBAR(); };
#pragma unroll
  for (int j = 0; j < 3; ++j) if (j < 2 || tid < (8 * A_KS - 32768) / 16) *(u32x4*)(lds + A_K + j * 16384 + tid * 16) = u32x4{0, 0, 0, 0};
  if (hf) {
    char* z = lds + A_UL + ch * A_ULS;
    *(u32x2*)(z + lane * 8) = u32x2{0, 0};
    if (lane < 4) *(u32x2*)(z + (lane == 0 ? 512 : 4608 + lane * 8)) = u32x2{0, 0};
  } else if (w < 6) {
    const int lo = (0x209072 >> (4 * w)) & 15, hi = (0x46F896 >> (4 * w)) & 15;
    const unsigned bits = (((2u << (hi - lo)) - 1u) << (idx + lo)) >> (8 * q);
    const unsigned val = w >= 4 ? 0xffffffffu : 0x3f803f80u;
    u32x4 f;
#pragma unroll
    for (int j = 0; j < 4; ++j)
      f[j] = plo((unsigned)__builtin_amdgcn_sbfe((int)bits, 2 * j, 1), (unsigned)__builtin_amdgcn_sbfe((int)bits, 2 * j + 1, 1)) & val;
    *(u32x4*)(lds + A_M + w * 1024 + lane * 16) = f;
  }
  if (U_P0 < 0) { __builtin_amdgcn_s_barrier(); load_u(0); }
  if (U_P1 < 0) load_u(1);
  PIN(pa); PIN(pi); PIN(pr); PIN(pc); PIN(pd); PIN(pk);
  u32x4 pf0, pf1;
  auto prefetch_w = [&]() {
    const int r8 = 256 * (w & 7);
    const int row = a == 0 ? 128 * hf + r8 : (hf ? 2048 - a - r8 : a + r8);
    const unsigned off = (row * 1026) & ~15;
    MEMBAR();
    pf0 = __builtin_amdgcn_raw_buffer_load_b128(mkrsrc(P.wr, 2048 * 513 * 2), lane * 16 + (lane == 63 ? 16 : 0), off, PF_AUX);
    pf1 = __builtin_amdgcn_raw_buffer_load_b128(mkrsrc(P.wi, 2048 * 513 * 2), lane * 16 + (lane == 63 ? 16 : 0), off, PF_AUX);
    MEMBAR();
  };
  if (W_PF == 1) prefetch_w();
  if (U_P0 == 0) load_u(0);
  if (W_PF == 2) prefetch_w();
  if (U_P1 == 0) load_u(1);
  char* st = lds + w * A_STS;
  const unsigned stb = __builtin_amdgcn_readfirstlane((unsigned)(size_t)st);
  f32x4 D[2][2];
  {
    const float dt = rbf(exp_acc(bflo(pd)));
    const float lr2 = rbf(exp_acc(bflo(pa))) * dt * -1.44269504088896341f;
    const float th = bflo(pi) * dt;

    const float ihi = 0.15915494309189535f, ilo = 6.4206382e-09f;
    const float rh = th * ihi, rl = __builtin_fmaf(th, ilo, __builtin_fmaf(th, ihi, -rh));
    const f32x2 z1 = cpow2(lr2, rh, rl, 1.f), z2 = csq(z1), z8 = csq(csq(z2));
    const f32x2 c32 = cpow2(lr2, rh, rl, 32.f);
    const char* sr = st + idx * 272 + q * 16;
    u32x4 Bf[2][4];
    f32x2 c4[4];
    c4[1] = z8; c4[2] = csq(z8); c4[3] = cmul(c4[2], z8);
    strow4<0, 272>(stb, 0x3f80u, pack2v(c4[1]), pack2v(c4[2]), pack2v(c4[3]));
    c4[0] = c32; c4[1] = cmul(c4[1], c32); c4[2] = cmul(c4[2], c32); c4[3] = cmul(c4[3], c32);
#define BSTEP(S, MORE) do { strow4<4 * S * 272, 272>(stb, pack2v(c4[0]), pack2v(c4[1]), pack2v(c4[2]), pack2v(c4[3])); \
      if (MORE) { c4[0] = cmul(c4[0], c32); c4[1] = cmul(c4[1], c32); c4[2] = cmul(c4[2], c32); c4[3] = cmul(c4[3], c32); } } while (0)
    BSTEP(1, 1); BSTEP(2, 1); BSTEP(3, 1);
#pragma unroll
    for (int ks = 0; ks < 4; ++ks) Bf[0][ks] = *(const u32x4*)(sr + ks * 64);
    const f32x2 z256 = csq(c4[0]);
    if (U_P0 == 1) load_u(0);
    if (U_P1 == 1) load_u(1);
    BSTEP(0, 1); BSTEP(1, 1); BSTEP(2, 1); BSTEP(3, 0);
#pragma unroll
    for (int ks = 0; ks < 4; ++ks) Bf[1][ks] = *(const u32x4*)(sr + ks * 64);

    if (U_P0 == 2) load_u(0);
    if (U_P1 == 2) load_u(1);
    const f32x2 c1 = z1, c2 = z2, c256 = z256, c512 = csq(z256);
    const f32x2 C = f32x2{bflo(pr), -bflo(pc)} * (dt + dt);
    f32x2 a0 = C, a1 = cmulc(C, c1), s0 = cmulc(C, c256);
    f32x2 a2 = cmulc(a0, c2), a3 = cmulc(a1, c2), s1 = cmulc(s0, c2);
    strow4<0, 272>(stb, pack2v(a0), pack2v(a1), pack2v(a2), pack2v(a3));
    a0 = cmulc(a2, c2); a1 = cmulc(a3, c2); f32x2 s2 = cmulc(s1, c2);
    a2 = cmulc(a0, c2); a3 = cmulc(a1, c2); f32x2 s3 = cmulc(s2, c2);
    strow4<4 * 272, 272>(stb, pack2v(a0), pack2v(a1), pack2v(a2), pack2v(a3));
    f32x2 s4 = cmulc(s3, c2), t516 = cmulc(s2, c256), t772 = cmulc(s2, c512);
    strow4<8 * 272, 272>(stb, pack2v(s0), pack2v(s1), pack2v(s2), pack2v(s3));
    strow1<12 * 272>(stb, pack2v(s4));
    const int rw0 = (int)((0x889AAABC01234567ull >> (4 * idx)) & 15);
    const char* p0 = st + rw0 * 272 + q * 16;
    u32x4 f0[4];
#pragma unroll
    for (int ks = 0; ks < 4; ++ks) f0[ks] = *(const u32x4*)(p0 + ks * 64);
    f32x2 t520 = cmulc(s4, c256), t776 = cmulc(s4, c512);
    strow4<0, 272>(stb, pack2v(t520), pack2v(t516), pack2v(t776), pack2v(t772));
    f32x2 e0 = cmulc(t520, c512), e1 = cmulc(t776, c512);
    f32x2 e2 = cmulc(e0, c512), e3 = cmulc(e1, c512);
    strow4<4 * 272, 272>(stb, pack2v(e0), pack2v(e1), pack2v(e2), pack2v(e3));
    const char* p1 = st + (idx & 7) * 272 + q * 16;
#pragma unroll
    for (int tb = 0; tb < 2; ++tb) { D[0][tb] = Z4; D[1][tb] = Z4; }
#pragma unroll
    for (int ks = 0; ks < 4; ++ks) {
      u32x4 f1 = *(const u32x4*)(p1 + ks * 64);
#pragma unroll
      for (int tb = 0; tb < 2; ++tb) { D[0][tb] = mfma16(f0[ks], Bf[tb][ks], D[0][tb]); D[1][tb] = mfma16(f1, Bf[tb][ks], D[1][tb]); }
    }
  }
  if (U_P0 == 3) load_u(0);
  if (U_P1 == 3) load_u(1);
  char* kk = lds + A_K + ch * A_KS;
  const char* ul = lds + A_UL + ch * A_ULS;
  const char* ub = ul + 512 + 32 * idx + 16 * q;
  const char* ml = lds + A_M + lane * 16;
  unsigned ka[2], kc[2];
  {
    if (hf) {
      *(f32x4*)(st + lane * 16) = D[0][0]; *(f32x4*)(st + 1024 + lane * 16) = D[0][1];
      D[0][0] = D[1][0]; D[0][1] = D[1][1];
    } else {
      *(f32x4*)(st + lane * 16) = D[1][0]; *(f32x4*)(st + 1024 + lane * 16) = D[1][1];
      D[0][0][3] += lane == 16 ? bflo(pk) : 0.f;
    }

    if (W_PF) asm volatile("" :: "v"(pf0), "v"(pf1));
    const unsigned base = __builtin_amdgcn_readfirstlane((unsigned)(size_t)lds + 40000u + 256u * wr);
    constexpr int O = A_UL + 520 - 40000;
    strow4<O, A_ULS>(base, plo(uv[0][0], uv[1][0]), phi(uv[0][0], uv[1][0]), plo(uv[0][1], uv[1][1]), phi(uv[0][1], uv[1][1]));
    strow4<O + 4 * A_ULS, A_ULS>(base, plo(uv[0][2], uv[1][2]), phi(uv[0][2], uv[1][2]), plo(uv[0][3], uv[1][3]), phi(uv[0][3], uv[1][3]));
    LDSBAR();

    if (hf) __builtin_amdgcn_s_setprio(Y_PRIO);
    const char* ps = lds + (w ^ 8) * A_STS + lane * 16;
#pragma unroll
    for (int tb = 0; tb < 2; ++tb) {
      f32x4 k = D[0][tb] + *(const f32x4*)(ps + tb * 1024);
      ka[tb] = pack2(k[0], k[1]); kc[tb] = pack2(k[2], k[3]);
      PIN(ka[tb]); PIN(kc[tb]);
    }
  }
  f32x4 acc[8];
#pragma unroll
  for (int t = 0; t < 8; ++t) acc[t] = Z4;
  if (hf == 0) {
    u32x4 H[17];
    {
      int xw = 2 * (531 - 7 - 128 - 4 - 256 * (q >> 1) - 8 * idx + 4 * (q & 1));
      PIN(xw);
#pragma unroll
      for (int tb = 0; tb < 2; ++tb) {
        char* p = kk + xw + 8 + 256 * (1 - tb);
        const unsigned mid = __builtin_amdgcn_alignbit(kc[tb], ka[tb], 16);
        *(u32x2*)p = u32x2{ka[tb], kc[tb]};
        *(unsigned*)(p + 2 * A_CS - 4) = ka[tb]; *(unsigned*)(p + 2 * A_CS) = kc[tb];
#pragma unroll
        for (int sft = 1; sft < 4; sft += 2) {
          char* po = p + sft * A_CS - 2 * sft;
          *(uint16_t*)po = (uint16_t)ka[tb]; *(unsigned*)(po + 2) = mid; *(uint16_t*)(po + 6) = (uint16_t)(kc[tb] >> 16);
        }
      }
      const int x0 = 527 - 512 - idx + 8 * q;
      int hp = (x0 & 3) * A_CS + (x0 & ~3) * 2, hp8 = hp + 8;
      PIN(hp); PIN(hp8);
      const u32x4 m511 = *(const u32x4*)(ml + 5120);
#pragma unroll
      for (int i = 0; i < 17; ++i) {
        const int m = i == 16 ? 0 : ((i & 1) ? 8 - (i >> 1) : 16 - (i >> 1));
        H[m] = ld16(kk + hp + 64 * (16 - m), kk + hp8 + 64 * (16 - m));
      }
      const unsigned k511 = __builtin_amdgcn_readlane(ka[1], 47) & 0xffffu;
      H[16] |= m511 & (k511 * 0x10001u);
    }

    u32x4 ring[PD + 1];
#pragma unroll
    for (int i = 0; i < PD; ++i) ring[i] = *(const u32x4*)(ub + 64 * (i - 8));
    SCHED();
    f32x4 part;
    const char* pt = lds + A_PT + ch * A_STS + lane * 16;
    char* yw = lds + (8 + ch) * A_STS + (ch & 3) * 32 + 32 * idx + 8 * q;
    static_for<-8, 57>([&](auto fc) {
      constexpr int f = decltype(fc)::value;
      if constexpr (f + PD <= 56) ring[(f + 8 + PD) % (PD + 1)] = *(const u32x4*)(ub + 64 * (f + PD));
      if constexpr ((f & 7) == 6 && f >= 14) {
        constexpr int al = (f + 2) / 8;
        part = *(const f32x4*)(pt + ((al >> 1) & 1) * (8 * A_STS) + (al & 1) * 1024);
      }
      static_for<0, 8>([&](auto ac) {
        constexpr int al = decltype(ac)::value, m = 8 * al - f;
        if constexpr (m >= 0 && m <= 16) acc[al] = mfma16(H[m], ring[(f + 8) % (PD + 1)], acc[al]);
      });
      if constexpr ((f > 0 && (f & 7) == 1) || f == 56) {
        constexpr int al = f / 8;
        f32x4 y = acc[al];
        if constexpr (al >= 2) y += part;
        *(u32x2*)(yw + (al & 3) * 512) = u32x2{pack2(y[0], y[1]), pack2(y[2], y[3])};
        if constexpr (al & 1) { LDSBAR(); }
      }
      SCHED();
    });
  } else {
    u32x4 T2[6], T3[4], M[5];
    {
      const unsigned k1024 = __builtin_amdgcn_readlane(kc[1], 15) & 0xffffu;
      const unsigned sa = q >= 2 ? ka[1] : ka[0];
      unsigned sc = q >= 2 ? kc[1] : kc[0];
      sc = lane == 47 ? sc & 0xffff0000u : sc;
      const int nb = 31 - 16 * (q >> 1) - idx;
      int a0 = (q & 1) ? A_R3 + 2 * (32 + nb) : A_R2 + 4 * (32 + nb);
      int a1 = a0 + ((q & 1) ? -64 : 2);
      PIN(a0); PIN(a1);
      *(uint16_t*)(kk + a0 + 128) = (uint16_t)sa; *(uint16_t*)(kk + a1 + 128) = (uint16_t)(sa >> 16);
      *(uint16_t*)(kk + a0) = (uint16_t)sc; *(uint16_t*)(kk + a1) = (uint16_t)(sc >> 16);
      int t2 = A_R2 + 8 * (15 - idx) + 16 * q, t3 = A_R3 + 4 * (15 - idx) + 16 * q, t28 = t2 + 8;
      PIN(t2); PIN(t3); PIN(t28);
#pragma unroll
      for (int i = 0; i < 5; ++i) M[i] = *(const u32x4*)(ml + 1024 * i);
#pragma unroll
      for (int dl = 3; dl <= 8; ++dl) T2[dl - 3] = ld16(kk + t2 + 64 * (8 - dl), kk + t28 + 64 * (8 - dl));
#pragma unroll
      for (int dl = 4; dl <= 7; ++dl) {
        const unsigned* p = (const unsigned*)(kk + t3 + 64 * (7 - dl));
        T3[dl - 4] = u32x4{p[0], p[1], p[2], p[3]};
      }
      M[4] &= k1024 * 0x10001u;
    }

    const char* wc = ul + 480 + 32 * idx + 16 * q;
    const char* wb = ul + 480 + 16 * idx + 16 * q;
    const char* yr = lds + 8 * A_STS + (idx >> 2) * (A_STS + 32) + (64 * ch + 16 * q + 4 * (idx & 3)) * 2;
    const unsigned eo = (64 * ch + lane) * 4096 + d0 * 2;
    const int slot = ch & 3;
#pragma unroll
    for (int c = 0; c < 5; ++c) {
      u32x4 ev;
      if (c > 0) {
        u32x2 e0 = trread(yr + ((c - 1) & 1) * 1024), e1 = trread(yr + ((c - 1) & 1) * 1024 + 4 * A_STS);
        ev = u32x4{e0[0], e0[1], e1[0], e1[1]};
      }
      auto emit = [&](auto kc_) { if (c > 0) store_if<decltype(kc_)::value>(ev, eo + (c - 1) * (512 * 4096), ro, slot); };
      if (c >= 3) ST128(ev, ro, eo + (c - 1) * (512 * 4096), 0);
      if (c < 3) {
        u32x4 wn[4], w3[2], uh[2];
#pragma unroll
        for (int i = 0; i < 4; ++i) wn[i] = *(const u32x4*)(wb + 256 * (4 * c + i));
#pragma unroll
        for (int i = 0; i < 2; ++i) {
          if (c > 0) w3[i] = *(const u32x4*)(wc + 512 * (2 * c - 2 + i));
          if (2 * c + 2 + i >= 3) uh[i] = *(const u32x4*)(ub + 64 * (8 * (2 * c + 2 + i) - 32));
        }
        u32x4 v[4], v3[2];
        emit(IC<0>{});
        SCHED();
#pragma unroll
        for (int i = 0; i < 4; ++i) v[i] = pack4(mfma16(wn[i], M[0], Z4), mfma16(wn[i], M[1], Z4));
        SCHED();
        emit(IC<1>{});
        if (c > 0) {
#pragma unroll
          for (int i = 0; i < 2; ++i) v3[i] = pack4(mfma16(w3[i], M[2], Z4), mfma16(w3[i], M[3], Z4));
        }
#pragma unroll
        for (int i = 0; i < 2; ++i) if (2 * c + 2 + i >= 3) acc[2 * c + 2 + i] = mfma16(M[4], uh[i], acc[2 * c + 2 + i]);
        SCHED();
        emit(IC<2>{});
#pragma unroll
        for (int i = 0; i < 4; ++i)
#pragma unroll
          for (int al = 2; al < 8; ++al) {
            const int dl = 2 * al - (4 * c + i);
            if (dl >= 3 && dl <= 8) acc[al] = mfma16(v[i], T2[dl - 3], acc[al]);
          }
        SCHED();
        emit(IC<3>{});
        if (c > 0) {
#pragma unroll
          for (int i = 0; i < 2; ++i)
#pragma unroll
            for (int al = 2 * c + 2 + i; al < 8; ++al) acc[al] = mfma16(v3[i], T3[al - (2 * c - 2 + i) - 4], acc[al]);
        }
        char* pt = lds + A_PT + ((c + 1) & 1) * (8 * A_STS) + ch * A_STS + lane * 16;
        *(f32x4*)pt = acc[2 * c + 2];
        *(f32x4*)(pt + 1024) = acc[2 * c + 3];
      }
      if (c < 4) { LDSBAR(); }
    }
  }

}

#if defined(__HIP_DEVICE_COMPILE__)
#define NO_LS_OPT __attribute__((target("no-load-store-opt")))
#else
#define NO_LS_OPT
#endif
__global__ __launch_bounds__(1024, 4) NO_LS_OPT void fused(Params P) {
  extern __shared__ __attribute__((aligned(16))) char lds[];
  const int b = blockIdx.x;
  if (b < 128) { if (ROLE_MASK & 1) role_spectral(P, lds, b); }
  else { if (ROLE_MASK & 2) role_ssm(P, lds, b - 128); }
}

extern "C" void kernel_launch(void* const* d_in, const int* in_sizes, int n_in,
                              void* d_out, int out_size, void* d_ws, size_t ws_size,
                              hipStream_t stream) {
  Params P;
  P.u = (const uint16_t*)d_in[0]; P.logA = (const uint16_t*)d_in[1]; P.Aim = (const uint16_t*)d_in[2]; P.Cr = (const uint16_t*)d_in[3];
  P.Ci = (const uint16_t*)d_in[4]; P.logdt = (const uint16_t*)d_in[5]; P.Dskip = (const uint16_t*)d_in[6]; P.x = (const uint16_t*)d_in[7];
  P.wr = (const uint16_t*)d_in[8]; P.wi = (const uint16_t*)d_in[9]; P.out = (uint16_t*)d_out; P.ws = (char*)d_ws;
  (void)hipFuncSetAttribute((const void*)fused, hipFuncAttributeMaxDynamicSharedMemorySize, LDS_BYTES);
  fused<<<256, 1024, LDS_BYTES, stream>>>(P);
}
```

```cpp
#include <hip/hip_runtime.h>
#include <hip/hip_bf16.h>
#include <stdint.h>

typedef __attribute__((ext_vector_type(8))) __bf16 bf16x8;
typedef __attribute__((ext_vector_type(2))) __bf16 bf16x2;
typedef __attribute__((ext_vector_type(2))) float f32x2;
typedef __attribute__((ext_vector_type(4))) float f32x4;
typedef __attribute__((ext_vector_type(4))) unsigned u32x4;
typedef __attribute__((ext_vector_type(2))) unsigned u32x2;
typedef __attribute__((ext_vector_type(4))) short s16x4;
#define LAS __attribute__((address_space(3)))
#define DI __device__ __forceinline__

#define MEMBAR() asm volatile("" ::: "memory")
#define PIN(x) asm volatile("" : "+v"(x))
#define PINF(f) do { PIN((f).re); PIN((f).im); } while (0)
#define SCHED() __builtin_amdgcn_sched_barrier(0)
#define PRIO(n) __builtin_amdgcn_s_setprio(n)
#define AUX_NT 2
#define AUX_SC1 16
typedef __amdgpu_buffer_rsrc_t rsrc_t;
DI rsrc_t mkrsrc(const void* p, unsigned bytes) { return __builtin_amdgcn_make_buffer_rsrc((void*)p, 0, bytes, 0x00020000); }
#define LD64S(r, off, soff, aux) __builtin_amdgcn_raw_buffer_load_b64(r, off, soff, aux)
#define ST64S(v, r, off, soff, aux) __builtin_amdgcn_raw_buffer_store_b64(v, r, off, soff, aux)
#define LDSBAR() do { asm volatile("s_waitcnt lgkmcnt(0)" ::: "memory"); __builtin_amdgcn_s_barrier(); MEMBAR(); } while (0)
#define LD32(r, off, aux) __builtin_amdgcn_raw_buffer_load_b32(r, off, 0, aux)
#define LD128(r, off, aux) __builtin_amdgcn_raw_buffer_load_b128(r, off, 0, aux)
#define ST32(v, r, off, aux) __builtin_amdgcn_raw_buffer_store_b32(v, r, off, 0, aux)
#define ST128(v, r, off, aux) __builtin_amdgcn_raw_buffer_store_b128(v, r, off, 0, aux)

DI unsigned pack2(float a, float b) { f32x2 v = {a, b}; bf16x2 r = __builtin_convertvector(v, bf16x2); return __builtin_bit_cast(unsigned, r); }
DI unsigned pack2v(f32x2 v) { bf16x2 r = __builtin_convertvector(v, bf16x2); return __builtin_bit_cast(unsigned, r); }
DI f32x4 mfma16(u32x4 a, u32x4 b, f32x4 c) {
  return __builtin_amdgcn_mfma_f32_16x16x32_bf16(__builtin_bit_cast(bf16x8, a), __builtin_bit_cast(bf16x8, b), c, 0, 0, 0);
}
DI float bflo(unsigned v) { return __uint_as_float(v << 16); }
DI float rbf(float x) { return bflo(pack2(x, 0.f)); }
DI float cosr(float rev) { return __builtin_amdgcn_cosf(rev); }
DI float sinr(float rev) { return __builtin_amdgcn_sinf(rev); }
DI u32x4 pack4(f32x4 re, f32x4 im) { return u32x4{pack2(re[0], im[0]), pack2(re[1], im[1]), pack2(re[2], im[2]), pack2(re[3], im[3])}; }
DI unsigned plo(unsigned a, unsigned b) { return __builtin_amdgcn_perm(b, a, 0x05040100u); }
DI unsigned phi(unsigned a, unsigned b) { return __builtin_amdgcn_perm(b, a, 0x07060302u); }
static constexpr f32x4 Z4 = {0.f, 0.f, 0.f, 0.f};

struct CFrag { u32x4 re, im; };
DI void setrot(CFrag& f, int j, float rev, unsigned im) {
  const unsigned p = pack2(cosr(rev), sinr(rev));
  f.re[j] = p; f.im[j] = __builtin_amdgcn_alignbit(p, p, 16) ^ im;
}
DI CFrag conjfrag(const CFrag& f) {
  CFrag g;
#pragma unroll
  for (int j = 0; j < 4; ++j) { g.re[j] = f.re[j] ^ 0x80000000u; g.im[j] = f.im[j] ^ 0x00008000u; }
  return g;
}

#define WS_F1 (4u << 20)
#define WS_EP (WS_F1 + 786432u)
#define WS_BYTES (5u << 20)
#define NREP 32
#define SPIN_CAP (1 << 18)
DI unsigned mktag(unsigned e) { return (e ^ 0x5bd1e995u) * 0x9E3779B1u + 0x7F4A7C15u; }

#define XS 72
#define ES 2560
#define L_R0 0
#define L_R1 40960
#define L_W 81920
#define WRS 1056
#define L_C (L_W + 33920)
#define A_STS 4352
#define A_UL (16 * A_STS)
#define A_ULS 4640
#define A_K (A_UL + 8 * A_ULS)
#define A_CS 1216
#define A_R2 (4 * A_CS)
#define A_R3 (A_R2 + 512)
#define A_KS 5696
#define A_M (A_K + 8 * A_KS)
#define A_PT 2176
#define LDS_BYTES (A_M + 6144)

struct Params {
  uint16_t* out;
  char* ws;
  const uint16_t *x, *wr, *wi, *u, *logA, *Aim, *Cr, *Ci, *logdt, *Dskip;
};
DI u32x2 ld8(const char* p) { return *(const u32x2*)p; }
DI void st8(char* p, u32x2 v) { *(u32x2*)p = v; }
DI u32x4 ld16(const char* p0, const char* p1) { const u32x2 lo = ld8(p0), hi = ld8(p1); return u32x4{lo[0], lo[1], hi[0], hi[1]}; }
DI u32x2 trread(const char* p) { return __builtin_bit_cast(u32x2, __builtin_amdgcn_ds_read_tr16_b64_v4i16((LAS s16x4*)p)); }
DI CFrag ldfrag(const char* p) { CFrag f; f.re = *(const u32x4*)p; f.im = *(const u32x4*)(p + 1024); return f; }

enum { C_GQ = 0, C_M1, C_TW0, C_TW1, C_G, C_M1C, C_TW1C, C_GC };

DI void dma16(rsrc_t r, unsigned voff, unsigned soff, unsigned ldsaddr) {
  asm volatile("s_mov_b32 m0, %3\n\ts_nop 0\n\tbuffer_load_dwordx4 %0, %1, %2 offen lds" :: "v"(voff), "s"(r), "s"(soff), "s"(ldsaddr) : "memory");
}
struct WSet { f32x4 r[2], i[2]; };
DI float ldbf(const char* p) { return bflo(*(const uint16_t*)p); }
static_assert(WRS == 1056, "offsets below");
template <bool MIRROR>
DI WSet read_filter(unsigned p1, unsigned p2, unsigned p3) {
  float t[16];
  if (MIRROR)
    asm volatile("ds_read_u16_d16_hi %0, %16 offset:0\n\tds_read_u16_d16_hi %1, %16 offset:1056\n\tds_read_u16_d16_hi %2, %16 offset:2112\n\tds_read_u16_d16_hi %3, %16 offset:3168\n\tds_read_u16_d16_hi %4, %17 offset:0\n\tds_read_u16_d16_hi %5, %18 offset:2112\n\tds_read_u16_d16_hi %6, %18 offset:1056\n\tds_read_u16_d16_hi %7, %18 offset:0\n\tds_read_u16_d16_hi %8, %16 offset:16896\n\tds_read_u16_d16_hi %9, %16 offset:17952\n\tds_read_u16_d16_hi %10, %16 offset:19008\n\tds_read_u16_d16_hi %11, %16 offset:20064\n\tds_read_u16_d16_hi %12, %17 offset:16896\n\tds_read_u16_d16_hi %13, %18 offset:19008\n\tds_read_u16_d16_hi %14, %18 offset:17952\n\tds_read_u16_d16_hi %15, %18 offset:16896\n\ts_waitcnt lgkmcnt(0)"
                 : "=&v"(t[0]), "=&v"(t[1]), "=&v"(t[2]), "=&v"(t[3]), "=&v"(t[4]), "=&v"(t[5]), "=&v"(t[6]), "=&v"(t[7]), "=&v"(t[8]), "=&v"(t[9]), "=&v"(t[10]), "=&v"(t[11]), "=&v"(t[12]), "=&v"(t[13]), "=&v"(t[14]), "=&v"(t[15]) : "v"(p1), "v"(p2), "v"(p3) : "memory");
  else
    asm volatile("ds_read_u16_d16_hi %0, %16 offset:0\n\tds_read_u16_d16_hi %1, %16 offset:1056\n\tds_read_u16_d16_hi %2, %16 offset:2112\n\tds_read_u16_d16_hi %3, %16 offset:3168\n\tds_read_u16_d16_hi %4, %17 offset:0\n\tds_read_u16_d16_hi %5, %17 offset:1056\n\tds_read_u16_d16_hi %6, %17 offset:2112\n\tds_read_u16_d16_hi %7, %17 offset:3168\n\tds_read_u16_d16_hi %8, %16 offset:16896\n\tds_read_u16_d16_hi %9, %16 offset:17952\n\tds_read_u16_d16_hi %10, %16 offset:19008\n\tds_read_u16_d16_hi %11, %16 offset:20064\n\tds_read_u16_d16_hi %12, %17 offset:16896\n\tds_read_u16_d16_hi %13, %17 offset:17952\n\tds_read_u16_d16_hi %14, %17 offset:19008\n\tds_read_u16_d16_hi %15, %17 offset:20064\n\ts_waitcnt lgkmcnt(0)"
                 : "=&v"(t[0]), "=&v"(t[1]), "=&v"(t[2]), "=&v"(t[3]), "=&v"(t[4]), "=&v"(t[5]), "=&v"(t[6]), "=&v"(t[7]), "=&v"(t[8]), "=&v"(t[9]), "=&v"(t[10]), "=&v"(t[11]), "=&v"(t[12]), "=&v"(t[13]), "=&v"(t[14]), "=&v"(t[15]) : "v"(p1), "v"(p2) : "memory");
  WSet s;
  s.r[0] = f32x4{t[0], t[1], t[2], t[3]}; s.r[1] = f32x4{t[4], t[5], t[6], t[7]};
  s.i[0] = f32x4{t[8], t[9], t[10], t[11]}; s.i[1] = f32x4{t[12], t[13], t[14], t[15]};
  return s;
}
struct Coef { f32x4 ar, ai, gr, gi, br, bi; };
DI Coef mkcoef(const WSet& s, float ss, float cc, float sc) {
  Coef c;
  c.ar = s.r[0] * ss + s.r[1] * cc; c.ai = s.i[0] * ss - s.i[1] * cc;
  c.gr = s.r[0] * cc + s.r[1] * ss; c.gi = s.i[0] * cc - s.i[1] * ss;
  c.br = (s.i[1] + s.i[0]) * -sc; c.bi = (s.r[0] - s.r[1]) * sc;
  return c;
}
#define WROW(r) ((r) * WRS + ((r) & 4) * 4)
#define IS 1040

DI void role_spectral(const Params& P, char* lds, const int b) {
  char* ws = P.ws;
  const int lane = threadIdx.x & 63, w = __builtin_amdgcn_readfirstlane(threadIdx.x >> 6), idx = lane & 15, q = lane >> 4;
  const int hb = w >> 3, w8 = w & 7;
  const rsrc_t rws = mkrsrc(ws, WS_BYTES);
  const rsrc_t rx = mkrsrc(P.x, 2048 * 1024 * 2);
  const rsrc_t rwr = mkrsrc(P.wr, 2048 * 513 * 2);
  const rsrc_t rwi = mkrsrc(P.wi, 2048 * 513 * 2);
  const int n2 = b & 7, cg = b >> 3;
  const bool special = b == 0;
  const unsigned lb = __builtin_amdgcn_readfirstlane((unsigned)(size_t)lds);
  auto wrow_of = [&](int r) { return special ? 128 * (r >> 3) + 256 * (r & 7) : ((r >> 3) ? 2048 - b - 256 * (r & 7) : b + 256 * (r & 7)); };
  auto load_filter = [&](int r) {
    const unsigned off = (wrow_of(r) * 1026) & ~15;
    const unsigned dst = lb + L_W + WROW(r);
    dma16(rwr, lane * 16, off, dst);
    dma16(rwi, lane * 16, off, dst + 16 * WRS);
  };
#define tailp ((const u32x4*)((const char*)((lane & 16) ? P.wi : P.wr) + ((wrow_of(idx) * 1026) & ~15) + 1024))
#define taild (lds + L_W + (q & 1) * (16 * WRS) + WROW(idx) + 1024)
  const unsigned xoff = ((64 * q + w) * 8 + n2) * 2048 + cg * 128 + idx * 8;
  unsigned toff = xoff ^ (w * 128);
  u32x2 v[4];
#pragma unroll
  for (int j = 0; j < 4; ++j) v[j] = LD64S(rx, xoff, j * 262144, AUX_NT);
  const unsigned ep = LD32(rws, WS_EP, AUX_SC1);
  const int krow = special ? 128 * hb : (hb ? 256 - b : b);
  unsigned poff = ((krow * 8 + w8) * 2048 + (64 * q + idx) * 8) ^ ((krow & 12) * 128);
  const int pj = (krow & 3);
  char* cl = lds + L_C + lane * 16;
  CFrag F;
  const float fidx = (float)idx, fq = (float)(4 * q);
  {
    const float d = fidx * (1.f / 16), r0 = d * fq;
#pragma unroll
    for (int j = 0; j < 4; ++j) setrot(F, j, r0 + (float)j * d, 0x8000u);
  }
  int ewr = (4 * q) * ES + (w + (w >> 2)) * 128 + idx * 8;
  int erd = w * ES + (5 * q) * 128 + idx * 8;
  CFrag A2;
  {
    const bool up = w + 16 * idx > 128;
    const float d = fidx * (1.f / 16) + (float)w * (1.f / 256);
    const float r0 = d * (fq + (float)n2 * 0.125f) - (up ? (float)n2 * 0.125f : 0.f);
    const unsigned im = up ? 0x80000000u : 0x8000u;
#pragma unroll
    for (int j = 0; j < 4; ++j) setrot(A2, j, r0 + (float)j * d, im);
  }
  PINF(A2); PIN(ewr); PIN(erd); PIN(toff);
  {
    PINF(F);
    SCHED();
    PRIO(3);
    u32x4 o[2];
#pragma unroll
    for (int tt = 0; tt < 2; ++tt) {
      u32x4 dd = {v[0][tt], v[1][tt], v[2][tt], v[3][tt]};
      o[tt] = pack4(mfma16(F.re, dd, Z4), mfma16(F.im, dd, Z4));
    }
#pragma unroll
    for (int r = 0; r < 4; ++r) st8(lds + L_R0 + ewr + r * ES, u32x2{o[0][r], o[1][r]});
    LDSBAR();
    u32x2 e[4];
#pragma unroll
    for (int j = 0; j < 4; ++j) e[j] = ld8(lds + L_R0 + erd + j * 128);
#pragma unroll
    for (int tt = 0; tt < 2; ++tt) {
      u32x4 vv = {e[0][tt], e[1][tt], e[2][tt], e[3][tt]};
      o[tt] = pack4(mfma16(A2.re, vv, Z4), mfma16(A2.im, vv, Z4));
    }
#pragma unroll
    for (int r = 0; r < 4; ++r) ST64S((u32x2{o[0][r] & ~1u, o[1][r]}), rws, toff, r * 262144, AUX_SC1);
  }
  PRIO(0);
  const unsigned eps = __builtin_amdgcn_readfirstlane(ep), tag = mktag(eps);
  MEMBAR();
  load_filter(w);
  if (w < 8) {
    const int e = (0x83783378 >> (4 * w)) & 15, h = (0x91 >> w) & 1, c = ((0x6A >> w) & 1) << (e - 1), d = w == C_GQ;
    const bool swap = w >= C_M1C, tw = (0x4C >> w) & 1;
    const float unit = ((0xA0 >> w) & 1) ? -1.f / 2048 : 1.f / 2048;
    u32x4 c0, c1;
#pragma unroll
    for (int j = 0; j < 4; ++j) {
      const int s = 4 * q + j, x = swap ? idx : s, y = swap ? s : idx;
      const int m = (((x >> h) << e) + c) * (y + d) + ((h & x & (y >> 3)) << 10);
      const float rev = (float)(m & 2047) * unit;
      const float cs = cosr(rev), sn = sinr(rev);
      unsigned p0 = pack2(cs, sn), p1 = pack2(-sn, cs);
      PIN(p0); PIN(p1);
      c0[j] = tw ? __float_as_uint(cs) : p0;
      c1[j] = tw ? __float_as_uint(sn) : p1;
    }
    *(u32x4*)(cl + w * 2048) = c0;
    *(u32x4*)(cl + w * 2048 + 1024) = c1;
    if (special && (w & 3) == 0) {
      const u32x4 mk = {0x80000000u, 0x8000u, 0x80000000u, 0x8000u};
      *(u32x4*)(cl + 16384 + w * 512) = c0 ^ mk;
      *(u32x4*)(cl + 16384 + w * 512 + 1024) = c1 ^ mk;
    }
  }
  const int kp = 4 * (w & 3) + (idx >> 2) + 64 * (w >> 2) + 16 * (idx & 3);
  const int kf = kp + 256 * (q >> 1);
  float ss, cc, sc;
  {
    const float nrm = 1.f / (2048.f * 512.f);
    const float th = (float)kf * (1.f / 1024);
    const float sn = sinr(th) * (0.5f * nrm);
    ss = 0.5f * nrm - sn; cc = 0.5f * nrm + sn; sc = cosr(th) * (0.5f * nrm);
  }
  int wa = hb * (256 * XS) + (idx + 64 * q) * XS + w8 * 8;
  int rp = kp * XS + q * 16, rp8 = rp + 8;
  int pp = idx * IS + (4 * (w & 3) + q) * 64 + ((w >> 2) ^ ((0x78 >> (2 * (w & 3))) & 3)) * 16;
  int rd = w * (2 * IS) + idx * 64 + (q ^ ((0x78 >> (2 * (idx >> 2))) & 3)) * 16;
  unsigned wp1 = lb + L_W + (q & 1) * WROW(4) + 2 * (b & 7) + 2 * kf, wp2 = lb + L_W + WROW(8) + (q & 1) * WROW(4) + 2 * (-b & 7) + 1024 - 2 * kf;
  auto patch_dc = [&](int rowp, int rowq, int lp, int lq) {
    char* pa = lds + L_W + ((lane >> 4) & 1) * (16 * WRS) + ((lane >> 3) & 1) * 1024;
    char* p1 = pa + WROW(rowp) + lp; char* p2 = pa + WROW(rowq) + lq;
    const float x1 = ldbf(p1), x2 = ldbf(p2), sg = (lane & 16) ? -0.5f : 0.5f;
    MEMBAR();
    *(uint16_t*)p1 = (uint16_t)pack2(0.5f * x1 + sg * x2, 0.f);
    *(uint16_t*)p2 = (uint16_t)pack2(0.5f * x2 + sg * x1, 0.f);
    MEMBAR();
  };
  PIN(ss); PIN(cc); PIN(sc);
  PIN(wa); PIN(poff); PIN(rp); PIN(rp8); PIN(pp); PIN(rd); PIN(wp1); PIN(wp2);
  Coef cf;
  f32x4 b2r, b2i;
  auto filter_setup = [&]() {
    if (special) {
      if (w == 0) { const int k2 = lane & 7; patch_dc(lane < 32 ? k2 : 8 + k2, lane < 32 ? (8 - k2) & 7 : 15 - k2, 0, 0); }
      const unsigned m0 = wp2 - WROW(8), m1 = lb + L_W + (1 - (q & 1)) * WROW(4) + 1024 - 2 * kf;
      cf = mkcoef(read_filter<true>(wp1, m0, m1 + WRS), ss, cc, sc);
      const Coef c2 = mkcoef(read_filter<true>(wp1 + WROW(8), m1 + WROW(8) + 3 * WRS, m1 + WROW(8)), ss, cc, sc);
      b2r = c2.br; b2i = c2.bi;
      PIN(b2r); PIN(b2i);
      wp1 = ((256 - kp) & 255) * XS + q * 16; wp2 = wp1 + 8;
      PIN(wp1); PIN(wp2);
      cf.gr = c2.ar; cf.gi = c2.ai;
    } else {
      if (w == 0) patch_dc(lane & 7, 8 + (lane & 7), 2 * (b & 7), 2 * (-b & 7));
      cf = mkcoef(read_filter<false>(wp1, wp2, 0), ss, cc, sc);
    }
    PIN(cf.ar); PIN(cf.ai); PIN(cf.gr); PIN(cf.gi); PIN(cf.br); PIN(cf.bi);
  };
  if (w == 8) {
    const u32x4 t = *tailp;
    asm volatile("s_waitcnt vmcnt(3)" ::: "memory");
    __builtin_amdgcn_s_barrier();
    *(u32x4*)taild = t;
  } else {
    asm volatile("s_waitcnt vmcnt(2)" ::: "memory");
    __builtin_amdgcn_s_barrier();
  }
  MEMBAR();
  if (threadIdx.x < NREP) ST32(tag, rws, WS_F1 + threadIdx.x * 512 + (n2 * 16 + cg) * 4, AUX_SC1);
  if (w == 0) asm volatile("s_waitcnt vmcnt(1)" ::: "memory"); else asm volatile("s_waitcnt vmcnt(0)" ::: "memory");
  LDSBAR();
  filter_setup();
  {
    const unsigned foff = WS_F1 + (b % NREP) * 512 + (w8 * 16 + idx) * 4;
    int spins = 0;
    for (;;) {
      MEMBAR();
      const unsigned f = LD32(rws, foff, AUX_SC1);
      if (__all(f == tag) || ++spins > SPIN_CAP) break;
      __builtin_amdgcn_s_sleep(1);
    }
    MEMBAR();
  }
  u32x2 d[4];
#pragma unroll
  for (int j = 0; j < 4; ++j) d[j] = LD64S(rws, poff, (j ^ pj) * 128, AUX_SC1);
  MEMBAR();
  CFrag M1 = ldfrag(cl + C_M1 * 2048), G = ldfrag(cl + C_G * 2048), M1c;
  f32x4 twc[3], tws[3];
#pragma unroll
  for (int c = 0; c < 2; ++c) { twc[c] = *(const f32x4*)(cl + (C_TW0 + c) * 2048); tws[c] = *(const f32x4*)(cl + (C_TW0 + c) * 2048 + 1024); }
  PINF(M1); PINF(G); PIN(twc[0]); PIN(twc[1]); PIN(tws[0]); PIN(tws[1]);
  {
    SCHED();
    PRIO(3);
    u32x4 o[2];
#pragma unroll
    for (int c = 0; c < 2; ++c) {
      u32x4 a = {d[0][c], d[1][c], d[2][c], d[3][c]};
      f32x4 dr = mfma16(a, F.re, Z4), di = mfma16(a, F.im, Z4);
      u32x4 bb = pack4(dr * twc[c] + di * tws[c], di * twc[c] - dr * tws[c]);
      o[c] = pack4(mfma16(c ? M1.re : F.re, bb, Z4), mfma16(c ? M1.im : F.im, bb, Z4));
    }
#pragma unroll
    for (int r = 0; r < 4; ++r) st8(lds + L_R1 + wa + 16 * r * XS, u32x2{o[0][r], o[1][r]});
  }
  CFrag Fc = conjfrag(F), Gc = ldfrag(cl + C_GC * 2048);
  PINF(Fc);
  LDSBAR();
  {
    f32x4 ar[2], ai[2];
#pragma unroll
    for (int tt = 0; tt < 2; ++tt) {
      u32x4 dd = ld16(lds + L_R1 + tt * (256 * XS) + rp, lds + L_R1 + tt * (256 * XS) + rp8);
      ar[tt] = mfma16(G.re, dd, Z4); ai[tt] = mfma16(G.im, dd, Z4);
    }
    u32x4 y[2];
    if (!special) {
      y[0] = pack4(cf.ar * ar[0] - cf.ai * ai[0] + cf.br * ar[1] - cf.bi * ai[1], cf.ar * ai[0] + cf.ai * ar[0] + cf.br * ai[1] + cf.bi * ar[1]);
      y[1] = pack4(cf.gr * ar[1] - cf.gi * ai[1] - cf.br * ar[0] + cf.bi * ai[0], cf.gr * ai[1] + cf.gi * ar[1] - cf.br * ai[0] - cf.bi * ar[0]);
    } else {
      u32x4 d0 = ld16(lds + L_R1 + wp1, lds + L_R1 + wp2), d1 = ld16(lds + L_R1 + 256 * XS + wp1, lds + L_R1 + 256 * XS + wp2);
      const CFrag Gm = ldfrag(cl + 16384 + 2048), Gq = ldfrag(cl + 16384);
      if (w == 0) { const unsigned m = idx == 0 ? 0x80008000u : 0u; d0[1] ^= m; d0[3] ^= m; d1[1] ^= m; d1[3] ^= m; }
      f32x4 br = mfma16(Gm.re, d0, Z4), bi = mfma16(Gm.im, d0, Z4);
      y[0] = pack4(cf.ar * ar[0] - cf.ai * ai[0] + cf.br * br - cf.bi * bi, cf.ar * ai[0] + cf.ai * ar[0] + cf.br * bi + cf.bi * br);
      br = mfma16(Gq.re, d1, Z4); bi = mfma16(Gq.im, d1, Z4);
      y[1] = pack4(cf.gr * ar[1] - cf.gi * ai[1] + b2r * br - b2i * bi, cf.gr * ai[1] + cf.gi * ar[1] + b2r * bi + b2i * br);
    }
    MEMBAR();
    M1c = ldfrag(cl + C_M1C * 2048);
    twc[2] = *(const f32x4*)(cl + C_TW1C * 2048); tws[2] = *(const f32x4*)(cl + C_TW1C * 2048 + 1024);
#pragma unroll
    for (int tt = 0; tt < 2; ++tt) *(u32x4*)(lds + L_R0 + tt * (16 * IS) + pp) = pack4(mfma16(y[tt], Gc.re, Z4), mfma16(y[tt], Gc.im, Z4));
  }
  LDSBAR();
  {
    u32x4 o[2];
#pragma unroll
    for (int c = 0; c < 2; ++c) {
      const u32x4 a = *(const u32x4*)(lds + L_R0 + rd + c * IS);
      f32x4 dr = mfma16(a, c ? M1c.re : Fc.re, Z4), di = mfma16(a, c ? M1c.im : Fc.im, Z4);
      u32x4 bb = pack4(dr * twc[2 * c] - di * tws[2 * c], di * twc[2 * c] + dr * tws[2 * c]);
      o[c] = pack4(mfma16(Fc.re, bb, Z4), mfma16(Fc.im, bb, Z4));
    }
#pragma unroll
    for (int r = 0; r < 4; ++r) ST64S((u32x2{o[0][r] | 1u, o[1][r]}), rws, poff, (r ^ pj) * 128, AUX_SC1);
  }
  PRIO(0);
  {
    CFrag A3;
    {
      int idx3 = idx, q3 = q;
      PIN(idx3); PIN(q3);
      const float g = (float)(8 * idx3 + n2) * (1.f / 2048), d = g * 16.f, r0 = g * (float)(w + 64 * q3), sh = (float)n2 * 0.125f;
#pragma unroll
      for (int j = 0; j < 4; ++j) {
        const bool up = j == 0 ? w + 64 * q3 > 128 : q3 >= 2;
        const float rev = r0 + (float)j * d;
        setrot(A3, j, up ? rev - sh : -rev, up ? 0x80000000u : 0x8000u);
      }
    }
    unsigned ooff = ((64 * q + w) * 8 + n2) * 4096 + 2048 + cg * 128 + idx * 8;
    PINF(A3); PIN(ooff);
    u32x2 d[4];
    int spins = 0;
    for (;;) {
      MEMBAR();
#pragma unroll
      for (int j = 0; j < 4; ++j) d[j] = LD64S(rws, toff, j * 262144, AUX_SC1);
      const unsigned all = d[0][0] & d[1][0] & d[2][0] & d[3][0];
      if (__all(all & 1u) || ++spins > SPIN_CAP) break;
      __builtin_amdgcn_s_sleep(2);
    }
    PRIO(3);
    u32x4 o[2];
#pragma unroll
    for (int tt = 0; tt < 2; ++tt) {
      u32x4 dd = {d[0][tt], d[1][tt], d[2][tt], d[3][tt]};
      o[tt] = pack4(mfma16(A3.re, dd, Z4), mfma16(A3.im, dd, Z4));
    }
#pragma unroll
    for (int r = 0; r < 4; ++r) st8(lds + L_R1 + ewr + r * ES, u32x2{o[0][r], o[1][r]});
    LDSBAR();
    if (w == 15) {
      if (lane < NREP) ST32(~tag, rws, WS_F1 + lane * 512 + (n2 * 16 + cg) * 4, AUX_SC1);
      if (b == 1 && lane == 0) ST32(eps + 1, rws, WS_EP, AUX_SC1);
    }
    u32x2 e[4];
#pragma unroll
    for (int j = 0; j < 4; ++j) e[j] = ld8(lds + L_R1 + erd + j * 128);
#pragma unroll
    for (int tt = 0; tt < 2; ++tt) {
      u32x4 vv = {e[0][tt], e[1][tt], e[2][tt], e[3][tt]};
      o[tt] = pack4(mfma16(Fc.re, vv, Z4), mfma16(Fc.im, vv, Z4));
    }
    const rsrc_t ro = mkrsrc(P.out, 2048 * 2048 * 2);
#pragma unroll
    for (int r = 0; r < 4; ++r) ST64S((u32x2{o[0][r], o[1][r]}), ro, ooff, r * 524288, AUX_SC1);
  }
}

template <int V> struct IC { static constexpr int value = V; };
template <int I, int N, class Fn>
DI void static_for(Fn&& fn) { if constexpr (I < N) { fn(IC<I>{}); static_for<I + 1, N>(fn); } }
DI f32x2 cmul(f32x2 a, f32x2 z) {
  f32x2 t, d;
  asm("v_pk_mul_f32 %0, %1, %2 op_sel_hi:[0,1]" : "=v"(t) : "v"(a), "v"(z));
  asm("v_pk_fma_f32 %0, %1, %2, %3 op_sel:[1,1,0] op_sel_hi:[1,0,1] neg_lo:[0,1,0]" : "=v"(d) : "v"(a), "v"(z), "v"(t));
  return d;
}
DI f32x2 cmulc(f32x2 a, f32x2 z) {
  f32x2 t, d;
  asm("v_pk_mul_f32 %0, %1, %2 op_sel_hi:[0,1] neg_hi:[0,1]" : "=v"(t) : "v"(a), "v"(z));
  asm("v_pk_fma_f32 %0, %1, %2, %3 op_sel:[1,1,0] op_sel_hi:[1,0,1]" : "=v"(d) : "v"(a), "v"(z), "v"(t));
  return d;
}
DI f32x2 csq(f32x2 a) { return cmul(a, a); }
DI f32x2 cpow2(float lr2, float rh, float rl, float e) {
  float m = __builtin_amdgcn_exp2f(lr2 * e);
  float fr = __builtin_fmaf(rl, e, __builtin_amdgcn_fractf(rh * e));
  return f32x2{cosr(fr), sinr(fr)} * m;
}
DI float exp_acc(float x) {
  const float t = x * 1.44269504088896341f;
  const float e = __builtin_fmaf(x, 1.44269504088896341f, -t) + x * 1.925963033500011e-08f;
  const float r = __builtin_amdgcn_exp2f(t);
  return __builtin_fmaf(r, e * 0.693147180559945f, r);
}
template <int O, int S>
DI void strow4(unsigned base, unsigned a, unsigned b, unsigned c, unsigned d) {
  asm volatile("s_mov_b32 m0, %4\n\ts_nop 0\n\tds_write_addtid_b32 %0 offset:%5\n\tds_write_addtid_b32 %1 offset:%6\n\t"
               "ds_write_addtid_b32 %2 offset:%7\n\tds_write_addtid_b32 %3 offset:%8"
               :: "v"(a), "v"(b), "v"(c), "v"(d), "s"(base), "i"(O), "i"(O + S), "i"(O + 2 * S), "i"(O + 3 * S) : "memory");
}
template <int O>
DI void strow1(unsigned base, unsigned a) {
  asm volatile("s_mov_b32 m0, %1\n\ts_nop 0\n\tds_write_addtid_b32 %0 offset:%2" :: "v"(a), "s"(base), "i"(O) : "memory");
}
#define PD 4

template <int K>
DI void store_if(u32x4 v, unsigned off, rsrc_t r, int slot) {
  asm volatile("s_cmp_lg_u32 %3, %4\n\ts_cbranch_scc1 .Lskip%=\n\tbuffer_store_dwordx4 %0, %1, %2, 0 offen\n\ts_nop 1\n.Lskip%=:"
               :: "v"(v), "v"(off), "s"(r), "s"(slot), "i"(K) : "scc");
}

DI void role_ssm(const Params& P, char* lds, const int a) {
  const int d0 = 8 * ((a & 7) * 16 + (a >> 3));
  const int tid = threadIdx.x, lane = tid & 63, w = __builtin_amdgcn_readfirstlane(tid >> 6), idx = lane & 15, q = lane >> 4;
  const int ch = w & 7, hf = w >> 3;
  const rsrc_t ru = mkrsrc(P.u, 2048 * 1024 * 2);
  const rsrc_t ro = mkrsrc(P.out, 2048 * 2048 * 2);
  const int pn = (d0 + ch) * 128 + hf * 64 + lane;
  const unsigned pa = P.logA[pn], pi = P.Aim[pn], pr = P.Cr[pn], pc = P.Ci[pn], pd = P.logdt[d0 + ch], pk = P.Dskip[d0 + ch];
  u32x4 uv[2];
  const int wr = (w + 2 * (a >> 3)) & 15;
  const unsigned uoff = (128 * wr + 2 * lane) * 2048 + d0 * 2;
  auto load_u = [&](int r) { MEMBAR(); uv[r] = LD128(ru, uoff + r * 2048, AUX_SC1); MEMBAR(); };
#pragma unroll
  for (int j = 0; j < 3; ++j) if (j < 2 || tid < (8 * A_KS - 32768) / 16) *(u32x4*)(lds + A_K + j * 16384 + tid * 16) = u32x4{0, 0, 0, 0};
  if (hf) {
    char* z = lds + A_UL + ch * A_ULS;
    *(u32x2*)(z + lane * 8) = u32x2{0, 0};
    if (lane < 4) *(u32x2*)(z + (lane == 0 ? 512 : 4608 + lane * 8)) = u32x2{0, 0};
  } else if (w < 6) {
    const int lo = (0x209072 >> (4 * w)) & 15, hi = (0x46F896 >> (4 * w)) & 15;
    const unsigned bits = (((2u << (hi - lo)) - 1u) << (idx + lo)) >> (8 * q);
    const unsigned val = w >= 4 ? 0xffffffffu : 0x3f803f80u;
    u32x4 f;
#pragma unroll
    for (int j = 0; j < 4; ++j)
      f[j] = plo((unsigned)__builtin_amdgcn_sbfe((int)bits, 2 * j, 1), (unsigned)__builtin_amdgcn_sbfe((int)bits, 2 * j + 1, 1)) & val;
    *(u32x4*)(lds + A_M + w * 1024 + lane * 16) = f;
  }
  int o_sr = w * A_STS + idx * 272 + q * 16, o_p0 = w * A_STS + (int)((0x889AAABC01234567ull >> (4 * idx)) & 15) * 272 + q * 16;
  int o_p1 = w * A_STS + (idx & 7) * 272 + q * 16, o_st = w * A_STS + lane * 16, o_ps = (w ^ 8) * A_STS + lane * 16;
  int o_w0, o_w1, o_r0, o_r1;
  if (hf == 0) {
    const int x0 = 15 - idx + 8 * q;
    o_w0 = 2 * (531 - 7 - 128 - 4 - 256 * (q >> 1) - 8 * idx + 4 * (q & 1)); o_w1 = 0;
    o_r0 = (x0 & 3) * A_CS + (x0 & ~3) * 2; o_r1 = 0;
  } else {
    const int nb = 31 - 16 * (q >> 1) - idx;
    o_w0 = (q & 1) ? A_R3 + 2 * (32 + nb) : A_R2 + 4 * (32 + nb); o_w1 = o_w0 + ((q & 1) ? -64 : 2) + A_K + ch * A_KS;
    o_r0 = A_R2 + 8 * (15 - idx) + 16 * q; o_r1 = A_R3 + 4 * (15 - idx) + 16 * q + A_K + ch * A_KS;
  }
  o_w0 += A_K + ch * A_KS; o_r0 += A_K + ch * A_KS;
  int o_r8 = o_r0 + 8, o_ub = A_UL + ch * A_ULS + 32 * idx + 16 * q;
  PIN(o_sr); PIN(o_p0); PIN(o_p1); PIN(o_st); PIN(o_ps); PIN(o_w0); PIN(o_w1); PIN(o_r0); PIN(o_r1); PIN(o_r8); PIN(o_ub);
  float fa = bflo(pa), fi = bflo(pi), fr = bflo(pr), fc = bflo(pc), fd = bflo(pd), fk = tid == 64 * ch + 16 ? bflo(pk) : 0.f;
  PIN(fa); PIN(fi); PIN(fr); PIN(fc); PIN(fd); PIN(fk);
  load_u(0);
  char* st = lds + w * A_STS;
  const unsigned stb = __builtin_amdgcn_readfirstlane((unsigned)(size_t)st);
  f32x4 D[2][2];
  {
    const float dt = rbf(exp_acc(fd));
    const float lr2 = rbf(exp_acc(fa)) * dt * -1.44269504088896341f;
    const float th = fi * dt;
    const float ihi = 0.15915494309189535f, ilo = 6.4206382e-09f;
    const float rh = th * ihi, rl = __builtin_fmaf(th, ilo, __builtin_fmaf(th, ihi, -rh));
    const f32x2 z1 = cpow2(lr2, rh, rl, 1.f), z2 = csq(z1), z8 = csq(csq(z2));
    const f32x2 c32 = cpow2(lr2, rh, rl, 32.f);
    const char* sr = lds + o_sr;
    u32x4 Bf[2][4];
    f32x2 c4[4];
    c4[1] = z8; c4[2] = csq(z8); c4[3] = cmul(c4[2], z8);
    strow4<0, 272>(stb, 0x3f80u, pack2v(c4[1]), pack2v(c4[2]), pack2v(c4[3]));
    c4[0] = c32; c4[1] = cmul(c4[1], c32); c4[2] = cmul(c4[2], c32); c4[3] = cmul(c4[3], c32);
#define BSTEP(S, MORE) do { strow4<4 * S * 272, 272>(stb, pack2v(c4[0]), pack2v(c4[1]), pack2v(c4[2]), pack2v(c4[3])); \
      if (MORE) { c4[0] = cmul(c4[0], c32); c4[1] = cmul(c4[1], c32); c4[2] = cmul(c4[2], c32); c4[3] = cmul(c4[3], c32); } } while (0)
    BSTEP(1, 1); BSTEP(2, 1); BSTEP(3, 1);
#pragma unroll
    for (int ks = 0; ks < 4; ++ks) Bf[0][ks] = *(const u32x4*)(sr + ks * 64);
    const f32x2 z256 = csq(c4[0]);
    BSTEP(0, 1); BSTEP(1, 1); BSTEP(2, 1); BSTEP(3, 0);
#pragma unroll
    for (int ks = 0; ks < 4; ++ks) Bf[1][ks] = *(const u32x4*)(sr + ks * 64);
    load_u(1);
    const f32x2 c1 = z1, c2 = z2, c256 = z256, c512 = csq(z256);
    const f32x2 C = {fr * (dt + dt), fc * -(dt + dt)};
    f32x2 a0 = C, a1 = cmulc(C, c1), s0 = cmulc(C, c256);
    f32x2 a2 = cmulc(a0, c2), a3 = cmulc(a1, c2), s1 = cmulc(s0, c2);
    strow4<0, 272>(stb, pack2v(a0), pack2v(a1), pack2v(a2), pack2v(a3));
    a0 = cmulc(a2, c2); a1 = cmulc(a3, c2); f32x2 s2 = cmulc(s1, c2);
    a2 = cmulc(a0, c2); a3 = cmulc(a1, c2); f32x2 s3 = cmulc(s2, c2);
    strow4<4 * 272, 272>(stb, pack2v(a0), pack2v(a1), pack2v(a2), pack2v(a3));
    f32x2 s4 = cmulc(s3, c2), t516 = cmulc(s2, c256), t772 = cmulc(s2, c512);
    strow4<8 * 272, 272>(stb, pack2v(s0), pack2v(s1), pack2v(s2), pack2v(s3));
    strow1<12 * 272>(stb, pack2v(s4));
    const char* p0 = lds + o_p0;
    u32x4 f0[4];
#pragma unroll
    for (int ks = 0; ks < 4; ++ks) f0[ks] = *(const u32x4*)(p0 + ks * 64);
    f32x2 t520 = cmulc(s4, c256), t776 = cmulc(s4, c512);
    strow4<0, 272>(stb, pack2v(t520), pack2v(t516), pack2v(t776), pack2v(t772));
    f32x2 e0 = cmulc(t520, c512), e1 = cmulc(t776, c512);
    f32x2 e2 = cmulc(e0, c512), e3 = cmulc(e1, c512);
    strow4<4 * 272, 272>(stb, pack2v(e0), pack2v(e1), pack2v(e2), pack2v(e3));
    const char* p1 = lds + o_p1;
#pragma unroll
    for (int tb = 0; tb < 2; ++tb) { D[0][tb] = Z4; D[1][tb] = Z4; }
#pragma unroll
    for (int ks = 0; ks < 4; ++ks) {
      u32x4 f1 = *(const u32x4*)(p1 + ks * 64);
#pragma unroll
      for (int tb = 0; tb < 2; ++tb) { D[0][tb] = mfma16(f0[ks], Bf[tb][ks], D[0][tb]); D[1][tb] = mfma16(f1, Bf[tb][ks], D[1][tb]); }
    }
  }
  const char* ul = lds + A_UL + ch * A_ULS;
  const char* ub = lds + o_ub + 512;
  const char* ml = lds + A_M + lane * 16;
  unsigned ka[2], kc[2];
  {
    f32x4 keep0, keep1;
    if (hf) {
      *(f32x4*)(lds + o_st) = D[0][0]; *(f32x4*)(lds + o_st + 1024) = D[0][1];
      keep0 = D[1][0]; keep1 = D[1][1];
    } else {
      *(f32x4*)(lds + o_st) = D[1][0]; *(f32x4*)(lds + o_st + 1024) = D[1][1];
      keep0 = D[0][0]; keep1 = D[0][1];
    }
    const unsigned base = __builtin_amdgcn_readfirstlane((unsigned)(size_t)lds + 40000u + 256u * wr);
    constexpr int O = A_UL + 520 - 40000;
    strow4<O, A_ULS>(base, plo(uv[0][0], uv[1][0]), phi(uv[0][0], uv[1][0]), plo(uv[0][1], uv[1][1]), phi(uv[0][1], uv[1][1]));
    strow4<O + 4 * A_ULS, A_ULS>(base, plo(uv[0][2], uv[1][2]), phi(uv[0][2], uv[1][2]), plo(uv[0][3], uv[1][3]), phi(uv[0][3], uv[1][3]));
    LDSBAR();
    if (hf) PRIO(2);
    keep0 += *(const f32x4*)(lds + o_ps); keep1 += *(const f32x4*)(lds + o_ps + 1024);
    ka[0] = pack2(keep0[0], keep0[1]); kc[0] = pack2(keep0[2], keep0[3] + fk);
    ka[1] = pack2(keep1[0], keep1[1]); kc[1] = pack2(keep1[2], keep1[3]);
    PIN(ka[0]); PIN(kc[0]); PIN(ka[1]); PIN(kc[1]);
  }
  f32x4 acc[8];
#pragma unroll
  for (int t = 0; t < 8; ++t) acc[t] = Z4;
  if (hf == 0) {
    u32x4 H[17];
    {
#pragma unroll
      for (int tb = 0; tb < 2; ++tb) {
        char* p = lds + o_w0 + 8 + 256 * (1 - tb);
        const unsigned mid = __builtin_amdgcn_alignbit(kc[tb], ka[tb], 16);
        *(u32x2*)p = u32x2{ka[tb], kc[tb]};
        *(unsigned*)(p + 2 * A_CS - 4) = ka[tb]; *(unsigned*)(p + 2 * A_CS) = kc[tb];
#pragma unroll
        for (int sft = 1; sft < 4; sft += 2) {
          char* po = p + sft * A_CS - 2 * sft;
          *(uint16_t*)po = (uint16_t)ka[tb]; *(unsigned*)(po + 2) = mid; *(uint16_t*)(po + 6) = (uint16_t)(kc[tb] >> 16);
        }
      }
      MEMBAR();
      const u32x4 m511 = *(const u32x4*)(ml + 5120);
#pragma unroll
      for (int i = 0; i < 17; ++i) {
        const int m = i == 16 ? 0 : ((i & 1) ? 8 - (i >> 1) : 16 - (i >> 1));
        H[m] = ld16(lds + o_r0 + 64 * (16 - m), lds + o_r8 + 64 * (16 - m));
      }
      const unsigned k511 = __builtin_amdgcn_readlane(ka[1], 47) & 0xffffu;
      H[16] |= m511 & (k511 * 0x10001u);
    }
    u32x4 ring[PD + 1];
#pragma unroll
    for (int i = 0; i < PD; ++i) ring[i] = *(const u32x4*)(ub + 64 * (i - 8));
    SCHED();
    f32x4 part;
    const char* pt = lds + A_PT + ch * A_STS + lane * 16;
    char* yw = lds + (8 + ch) * A_STS + (ch & 3) * 32 + 32 * idx + 8 * q;
    static_for<-8, 57>([&](auto fc) {
      constexpr int f = decltype(fc)::value;
      if constexpr (f + PD <= 56) ring[(f + 8 + PD) % (PD + 1)] = *(const u32x4*)(ub + 64 * (f + PD));
      if constexpr ((f & 7) == 6 && f >= 14) {
        constexpr int al = (f + 2) / 8;
        part = *(const f32x4*)(pt + ((al >> 1) & 1) * (8 * A_STS) + (al & 1) * 1024);
      }
      static_for<0, 8>([&](auto ac) {
        constexpr int al = decltype(ac)::value, m = 8 * al - f;
        if constexpr (m >= 0 && m <= 16) acc[al] = mfma16(H[m], ring[(f + 8) % (PD + 1)], acc[al]);
      });
      if constexpr ((f > 0 && (f & 7) == 1) || f == 56) {
        constexpr int al = f / 8;
        f32x4 y = acc[al];
        if constexpr (al >= 2) y += part;
        *(u32x2*)(yw + (al & 3) * 512) = u32x2{pack2(y[0], y[1]), pack2(y[2], y[3])};
        if constexpr (al & 1) { LDSBAR(); }
      }
      SCHED();
    });
  } else {
    u32x4 T2[6], T3[4], M[5];
    {
      const unsigned k1024 = __builtin_amdgcn_readlane(kc[1], 15) & 0xffffu;
      const unsigned sa = q >= 2 ? ka[1] : ka[0];
      unsigned sc = q >= 2 ? kc[1] : kc[0];
      sc = lane == 47 ? sc & 0xffff0000u : sc;
      *(uint16_t*)(lds + o_w0 + 128) = (uint16_t)sa; *(uint16_t*)(lds + o_w1 + 128) = (uint16_t)(sa >> 16);
      *(uint16_t*)(lds + o_w0) = (uint16_t)sc; *(uint16_t*)(lds + o_w1) = (uint16_t)(sc >> 16);
      MEMBAR();
#pragma unroll
      for (int i = 0; i < 5; ++i) M[i] = *(const u32x4*)(ml + 1024 * i);
#pragma unroll
      for (int dl = 3; dl <= 8; ++dl) T2[dl - 3] = ld16(lds + o_r0 + 64 * (8 - dl), lds + o_r8 + 64 * (8 - dl));
#pragma unroll
      for (int dl = 4; dl <= 7; ++dl) {
        const unsigned* p = (const unsigned*)(lds + o_r1 + 64 * (7 - dl));
        T3[dl - 4] = u32x4{p[0], p[1], p[2], p[3]};
      }
      M[4] &= k1024 * 0x10001u;
    }
    const char* wc = lds + o_ub + 480;
    const char* wb = ul + 480 + 16 * idx + 16 * q;
    const char* yr = lds + 8 * A_STS + (idx >> 2) * (A_STS + 32) + (64 * ch + 16 * q + 4 * (idx & 3)) * 2;
    const unsigned eo = (64 * ch + lane) * 4096 + d0 * 2;
    const int slot = ch & 3;
#pragma unroll
    for (int c = 0; c < 5; ++c) {
      u32x4 ev;
      if (c > 0) {
        u32x2 e0 = trread(yr + ((c - 1) & 1) * 1024), e1 = trread(yr + ((c - 1) & 1) * 1024 + 4 * A_STS);
        ev = u32x4{e0[0], e0[1], e1[0], e1[1]};
      }
      auto emit = [&](auto kc_) { if (c > 0) store_if<decltype(kc_)::value>(ev, eo + (c - 1) * (512 * 4096), ro, slot); };
      if (c >= 3) ST128(ev, ro, eo + (c - 1) * (512 * 4096), 0);
      if (c < 3) {
        u32x4 wn[4], w3[2], uh[2];
#pragma unroll
        for (int i = 0; i < 4; ++i) wn[i] = *(const u32x4*)(wb + 256 * (4 * c + i));
#pragma unroll
        for (int i = 0; i < 2; ++i) {
          if (c > 0) w3[i] = *(const u32x4*)(wc + 512 * (2 * c - 2 + i));
          if (2 * c + 2 + i >= 3) uh[i] = *(const u32x4*)(ub + 64 * (8 * (2 * c + 2 + i) - 32));
        }
        u32x4 v[4], v3[2];
        emit(IC<0>{});
        SCHED();
#pragma unroll
        for (int i = 0; i < 4; ++i) v[i] = pack4(mfma16(wn[i], M[0], Z4), mfma16(wn[i], M[1], Z4));
        SCHED();
        emit(IC<1>{});
        if (c > 0) {
#pragma unroll
          for (int i = 0; i < 2; ++i) v3[i] = pack4(mfma16(w3[i], M[2], Z4), mfma16(w3[i], M[3], Z4));
        }
#pragma unroll
        for (int i = 0; i < 2; ++i) if (2 * c + 2 + i >= 3) acc[2 * c + 2 + i] = mfma16(M[4], uh[i], acc[2 * c + 2 + i]);
        SCHED();
        emit(IC<2>{});
#pragma unroll
        for (int i = 0; i < 4; ++i)
#pragma unroll
          for (int al = 2; al < 8; ++al) {
            const int dl = 2 * al - (4 * c + i);
            if (dl >= 3 && dl <= 8) acc[al] = mfma16(v[i], T2[dl - 3], acc[al]);
          }
        SCHED();
        emit(IC<3>{});
        if (c > 0) {
#pragma unroll
          for (int i = 0; i < 2; ++i)
#pragma unroll
            for (int al = 2 * c + 2 + i; al < 8; ++al) acc[al] = mfma16(v3[i], T3[al - (2 * c - 2 + i) - 4], acc[al]);
        }
        char* pt = lds + A_PT + ((c + 1) & 1) * (8 * A_STS) + ch * A_STS + lane * 16;
        *(f32x4*)pt = acc[2 * c + 2];
        *(f32x4*)(pt + 1024) = acc[2 * c + 3];
      }
      if (c < 4) { LDSBAR(); }
    }
  }
}

#if defined(__HIP_DEVICE_COMPILE__)
#define NO_LS_OPT __attribute__((target("no-load-store-opt")))
#else
#define NO_LS_OPT
#endif
__global__ __launch_bounds__(1024, 4) NO_LS_OPT void fused(Params P) {
  extern __shared__ __attribute__((aligned(16))) char lds[];
  const int b = blockIdx.x;
  if (b < 128) role_spectral(P, lds, b);
  else role_ssm(P, lds, b - 128);
}

extern "C" void kernel_launch(void* const* d_in, const int* in_sizes, int n_in,
                              void* d_out, int out_size, void* d_ws, size_t ws_size,
                              hipStream_t stream) {
  Params P;
  P.u = (const uint16_t*)d_in[0]; P.logA = (const uint16_t*)d_in[1]; P.Aim = (const uint16_t*)d_in[2]; P.Cr = (const uint16_t*)d_in[3];
  P.Ci = (const uint16_t*)d_in[4]; P.logdt = (const uint16_t*)d_in[5]; P.Dskip = (const uint16_t*)d_in[6]; P.x = (const uint16_t*)d_in[7];
  P.wr = (const uint16_t*)d_in[8]; P.wi = (const uint16_t*)d_in[9]; P.out = (uint16_t*)d_out; P.ws = (char*)d_ws;
  (void)hipFuncSetAttribute((const void*)fused, hipFuncAttributeMaxDynamicSharedMemorySize, LDS_BYTES);
  fused<<<256, 1024, LDS_BYTES, stream>>>(P);
}
```
